```python
import torch

def _softmax(x, axis=-1):
    return torch.softmax(x, dim=axis)
def _gelu(x):
    return torch.nn.functional.gelu(x, approximate="tanh")
def _silu(x):
    return torch.nn.functional.silu(x)
def _sigmoid(x):
    return torch.sigmoid(x)
def _layernorm(x, axis=-1, eps=1e-5):
    m = x.mean(dim=axis, keepdim=True)
    v = x.var(dim=axis, keepdim=True, unbiased=False)
    return (x - m) / torch.sqrt(v + eps)
def _scatter_add(tgt, idx, src, axis=0):
    return tgt.clone().index_add_(axis, idx, src)
def _segment_sum(src, seg, n):
    out = torch.zeros((n,) + src.shape[1:], dtype=src.dtype)
    return out.index_add_(0, seg, src)
def _pad(x, pw):
    flat = tuple(v for pair in reversed(pw) for v in pair)
    return torch.nn.functional.pad(x, flat)

kernel_name = 'parallel_sum__compressed_kv_cla__glu_gated_reglu__prompt'

def setup_inputs():

    _w = (lambda a: (lambda t: t.to(torch.bfloat16) if t.is_floating_point() else t)(torch.as_tensor(a)))
    def _A_setup():
        B, H, Sq, Sk, D, M = 1, 32, 2048, 2048, 128, 512
        Dc = 4 * D
        return {
            "x": _w((lambda *s: torch.randn(*s, dtype=torch.bfloat16) * 0.05)(B, Sq, M)),
            "c_kv": _w((lambda *s: torch.randn(*s, dtype=torch.bfloat16) * 0.4)(B, Sk, Dc)),
            "w_q": _w((lambda *s: torch.randn(*s, dtype=torch.bfloat16) * 1.6)(M, H, D) * (M ** -0.5)),
            "w_uk": _w((lambda *s: torch.randn(*s, dtype=torch.bfloat16) * 1.6)(Dc, H, D) * (Dc ** -0.5)),
            "w_uv": _w((lambda *s: torch.randn(*s, dtype=torch.bfloat16) * 0.4)(Dc, H, D) * (Dc ** -0.5)),
            "w_o": _w((lambda *s: torch.randn(*s, dtype=torch.bfloat16) * 0.4)(H, D, M) * ((H * D) ** -0.5)),
        }
    _A_in = _A_setup()

    def _B_setup():
        B, S, D, F = 1, 2048, 512, 1376
        return {
            "x": _w((lambda *s: torch.randn(*s, dtype=torch.bfloat16) * 0.05)(B, S, D)),
            "W_gate": _w((lambda *s: torch.randn(*s, dtype=torch.bfloat16) * 0.2)(D, F) * (2.0 / D) ** 0.5),
            "W_up": _w((lambda *s: torch.randn(*s, dtype=torch.bfloat16) * 0.2)(D, F) * (2.0 / D) ** 0.5),
            "W_down": _w((lambda *s: torch.randn(*s, dtype=torch.bfloat16) * 0.2)(F, D) * (1.0 / F) ** 0.5),
        }
    _B_in = _B_setup()

    return {
        "A_x": _A_in["x"],
        "A_c_kv": _A_in["c_kv"],
        "A_w_q": _A_in["w_q"],
        "A_w_uk": _A_in["w_uk"],
        "A_w_uv": _A_in["w_uv"],
        "A_w_o": _A_in["w_o"],
        "B_x": _B_in["x"],
        "B_W_gate": _B_in["W_gate"],
        "B_W_up": _B_in["W_up"],
        "B_W_down": _B_in["W_down"],
    }

def reference(A_x, A_c_kv, A_w_q, A_w_uk, A_w_uv, A_w_o, B_x, B_W_gate, B_W_up, B_W_down):
    def _A_ref(x, c_kv, w_q, w_uk, w_uv, w_o):
        B, Sq, M = x.shape[0], x.shape[1], x.shape[2]
        Sk = c_kv.shape[1]
        H, D = w_q.shape[1], w_q.shape[2]
        q = torch.einsum("bqm,mhd->bhqd", x, w_q)
        k = torch.einsum("bkc,chd->bhkd", c_kv, w_uk)
        v = torch.einsum("bkc,chd->bhkd", c_kv, w_uv)
        scores = torch.einsum("bhqd,bhkd->bhqk", q, k) * (D ** -0.5)
        off = Sk - Sq
        qi = torch.arange(Sq)[:, None]
        kj = torch.arange(Sk)[None, :]
        cmask = kj <= (qi + off)
        scores = torch.where(cmask[None, None], scores, -1e9)
        p = _softmax(scores, axis=-1)
        ctx = torch.einsum("bhqk,bhkd->bhqd", p, v)
        return torch.einsum("bhqd,hdm->bqm", ctx, w_o)

    def _B_ref(x, W_gate, W_up, W_down):
        gate = torch.einsum("bsd,df->bsf", x, W_gate)
        up = torch.einsum("bsd,df->bsf", x, W_up)
        gate = gate * (lambda x, dt: x.to(dt))(gate > 0, gate.dtype)
        h = gate * up
        return torch.einsum("bsf,fd->bsd", h, W_down)

    x = A_x
    xn = _layernorm(x, axis=-1)
    return x + _A_ref(xn, A_c_kv, A_w_q, A_w_uk, A_w_uv, A_w_o) + _B_ref(xn, B_W_gate, B_W_up, B_W_down)
```

```cpp
#include <hip/hip_runtime.h>
#include <hip/hip_bf16.h>
#include <cstdint>
typedef unsigned short u16;
__device__ inline float b2f(u16 b) { return __uint_as_float(((unsigned)b) << 16); }
__device__ inline u16 f2b(float f) { unsigned u = __float_as_uint(f); u += 0x7FFFu + ((u >> 16) & 1u); return (u16)(u >> 16); }
namespace nv {
constexpr int S = 2048, M = 512, H = 32, D = 128, F = 1376;
__global__ void ln_k(const u16* x, float* xn) {
    int row = blockIdx.x; int t = threadIdx.x;
    float v[8]; float s = 0;
    for (int j = 0; j < 8; ++j) { v[j] = b2f(x[row * M + t * 8 + j]); s += v[j]; }
    for (int o = 32; o >= 1; o >>= 1) s += __shfl_xor(s, o);
    float m = s / M; float q = 0;
    for (int j = 0; j < 8; ++j) { float d = v[j] - m; q += d * d; }
    for (int o = 32; o >= 1; o >>= 1) q += __shfl_xor(q, o);
    float r = 1.0f / sqrtf(q / M + 1e-5f);
    for (int j = 0; j < 8; ++j) xn[row * M + t * 8 + j] = (v[j] - m) * r;
}
__global__ void cvt_k(const u16* a, float* o, int n) { int i = blockIdx.x * 256 + threadIdx.x; if (i < n) o[i] = b2f(a[i]); }
__global__ void gemm_k(const float* A, const u16* B, float* C, int Mm, int N, int K, int lda, int ldc, int accum) {
    __shared__ float As[16][17], Bs[16][17];
    int tx = threadIdx.x & 15, ty = threadIdx.x >> 4;
    int row = blockIdx.y * 16 + ty, col = blockIdx.x * 16 + tx;
    float acc = 0;
    for (int k0 = 0; k0 < K; k0 += 16) {
        As[ty][tx] = A[(size_t)row * lda + k0 + tx];
        Bs[ty][tx] = b2f(B[(size_t)(k0 + ty) * N + col]);
        __syncthreads();
        for (int k = 0; k < 16; ++k) acc += As[ty][k] * Bs[k][tx];
        __syncthreads();
    }
    if (accum) acc += C[(size_t)row * ldc + col];
    C[(size_t)row * ldc + col] = acc;
}
__global__ __launch_bounds__(128) void attn_k(const float* q, const float* k, const float* v, float* ctx) {
    int row = blockIdx.x, h = blockIdx.y, t = threadIdx.x;
    __shared__ float sc[2048]; __shared__ float qs[128]; __shared__ float red[128];
    qs[t] = q[(size_t)row * 4096 + h * 128 + t]; __syncthreads();
    float mx = -1e30f;
    for (int j = t; j <= row; j += 128) { float s = 0; const float* kr = k + (size_t)j * 4096 + h * 128; _Pragma("unroll 4") for (int d = 0; d < 128; ++d) s += qs[d] * kr[d]; s *= 0.08838834764831845f; sc[j] = s; mx = fmaxf(mx, s); }
    red[t] = mx; __syncthreads();
    for (int o = 64; o >= 1; o >>= 1) { if (t < o) red[t] = fmaxf(red[t], red[t + o]); __syncthreads(); }
    mx = red[0]; __syncthreads();
    float sum = 0;
    for (int j = t; j <= row; j += 128) { float e = expf(sc[j] - mx); sc[j] = e; sum += e; }
    red[t] = sum; __syncthreads();
    for (int o = 64; o >= 1; o >>= 1) { if (t < o) red[t] += red[t + o]; __syncthreads(); }
    sum = red[0];
    float acc = 0;
    for (int j = 0; j <= row; ++j) acc += sc[j] * v[(size_t)j * 4096 + h * 128 + t];
    ctx[(size_t)row * 4096 + h * 128 + t] = acc / sum;
}
__global__ void glu_k(const float* g, const float* u, float* hh, int n) { int i = blockIdx.x * 256 + threadIdx.x; if (i < n) hh[i] = (g[i] > 0 ? g[i] : 0.f) * u[i]; }
__global__ void fin_k(const u16* x, const float* o, u16* out, float* outf, int n) { int i = blockIdx.x * 256 + threadIdx.x; if (i < n) { float r = b2f(x[i]) + o[i]; if (out) out[i] = f2b(r); if (outf) outf[i] = r; } }

struct Bufs { float *xn, *c, *q, *k, *v, *ctx, *g, *u, *h, *o; };
inline Bufs carve(void* ws) {
    float* p = (float*)ws; Bufs b;
    b.xn = p; p += S * M; b.c = p; p += S * M; b.q = p; p += S * 4096; b.k = p; p += S * 4096; b.v = p; p += S * 4096; b.ctx = p; p += S * 4096;
    b.g = p; p += S * F; b.u = p; p += S * F; b.h = p; p += S * F; b.o = p; p += S * M; return b;
}
inline void run(void* const* d_in, u16* out, float* outf, void* ws, hipStream_t st) {
    Bufs b = carve(ws);
    const u16* x = (const u16*)d_in[0]; const u16* c = (const u16*)d_in[1]; const u16* wq = (const u16*)d_in[2]; const u16* wk = (const u16*)d_in[3];
    const u16* wv = (const u16*)d_in[4]; const u16* wo = (const u16*)d_in[5]; const u16* wg = (const u16*)d_in[7]; const u16* wu = (const u16*)d_in[8]; const u16* wd = (const u16*)d_in[9];
    ln_k<<<S, 64, 0, st>>>(x, b.xn);
    cvt_k<<<S * M / 256, 256, 0, st>>>(c, b.c, S * M);
    gemm_k<<<dim3(4096 / 16, S / 16), 256, 0, st>>>(b.xn, wq, b.q, S, 4096, 512, 512, 4096, 0);
    gemm_k<<<dim3(4096 / 16, S / 16), 256, 0, st>>>(b.c, wk, b.k, S, 4096, 512, 512, 4096, 0);
    gemm_k<<<dim3(4096 / 16, S / 16), 256, 0, st>>>(b.c, wv, b.v, S, 4096, 512, 512, 4096, 0);
    attn_k<<<dim3(S, H), 128, 0, st>>>(b.q, b.k, b.v, b.ctx);
    gemm_k<<<dim3(F / 16, S / 16), 256, 0, st>>>(b.xn, wg, b.g, S, F, 512, 512, F, 0);
    gemm_k<<<dim3(F / 16, S / 16), 256, 0, st>>>(b.xn, wu, b.u, S, F, 512, 512, F, 0);
    glu_k<<<S * F / 256, 256, 0, st>>>(b.g, b.u, b.h, S * F);
    gemm_k<<<dim3(M / 16, S / 16), 256, 0, st>>>(b.ctx, wo, b.o, S, M, 4096, 4096, M, 0);
    gemm_k<<<dim3(M / 16, S / 16), 256, 0, st>>>(b.h, wd, b.o, S, M, F, F, M, 1);
    fin_k<<<S * M / 256, 256, 0, st>>>(x, b.o, out, outf, S * M);
}
}
extern "C" void kernel_launch(void* const* d_in, const int* in_sizes, int n_in, void* d_out, int out_size, void* d_ws, size_t ws_size, hipStream_t stream) {
    nv::run(d_in, (u16*)d_out, nullptr, d_ws, stream);
}
```

```cpp
#include <hip/hip_runtime.h>
#include <hip/hip_bf16.h>
#include <cstdint>

typedef unsigned short u16;
typedef __bf16 bf16x8 __attribute__((ext_vector_type(8)));
typedef __bf16 bf16x4 __attribute__((ext_vector_type(4)));
typedef __bf16 bf16x2 __attribute__((ext_vector_type(2)));
typedef short s16x4 __attribute__((ext_vector_type(4)));
typedef float f32x4 __attribute__((ext_vector_type(4)));
typedef float f32x2 __attribute__((ext_vector_type(2)));
typedef unsigned u32x4 __attribute__((ext_vector_type(4)));
typedef unsigned u32x2 __attribute__((ext_vector_type(2)));
#define LAS __attribute__((address_space(3)))
typedef LAS char* lptr;
#define DI __device__ __forceinline__
#define SBAR() __builtin_amdgcn_sched_barrier(0)
#define MFMA(a, b, c) __builtin_amdgcn_mfma_f32_16x16x32_bf16((a), (b), (c), 0, 0, 0)

constexpr int DM = 512, FF = 1376;
constexpr int CTX_KS = 176;
constexpr size_t WS_XN = 0;
constexpr size_t WS_K = WS_XN + (size_t)128 * 16 * 1024;
constexpr size_t WS_V = WS_K + (size_t)32 * 512 * 1024;
constexpr size_t WS_CTX = WS_V + (size_t)32 * 512 * 1024;
constexpr size_t WS_PART = WS_CTX + (size_t)128 * CTX_KS * 1024 + 65536;
constexpr size_t WS_FLAG = WS_PART + (size_t)64 * 4 * 32768;
constexpr size_t WS_CTR = WS_FLAG + 64 * 512;
constexpr size_t WS_LNF = WS_CTR + 256;
constexpr size_t WS_KVF = WS_LNF + 2048;
constexpr size_t WS_F1 = WS_KVF + 32 * 128;
constexpr size_t WS_F2 = WS_F1 + 2048;
constexpr size_t WS_END = WS_F2 + 2048;
constexpr float QSCALE = 0.08838834764831845f * 1.4426950408889634f;

#define ATTN_ASM \
    "s_mov_b64 s[36:37], %0\n\t" \
    "s_mov_b64 s[38:39], %1\n\t" \
    "s_mov_b64 s[40:41], %2\n\t" \
    "s_mov_b64 s[42:43], %3\n\t" \
    "s_lshl_b32 s44, %4, 1\n\t" \
    "s_add_u32 s44, s44, 2\n\t" \
    "s_lshr_b32 s45, %5, 2\n\t" \
    "s_and_b32 s46, %5, 3\n\t" \
    "s_mov_b32 s49, %6\n\t" \
    "s_cmp_eq_u32 s45, 0\n\t" \
    "s_cselect_b32 s72, s44, 99\n\t" \
    "s_mov_b32 s47, 0\n\t" \
    "s_sub_u32 s78, s38, s36\n\t" \
    "s_subb_u32 s79, s39, s37\n\t" \
    "s_add_u32 s58, s38, 0x4000\n\t" \
    "s_addc_u32 s59, s39, 0\n\t" \
    "s_cmp_eq_u32 s44, 2\n\t" \
    "s_cselect_b32 s52, s45, 0\n\t" \
    "s_add_u32 s52, s52, 2\n\t" \
    "s_lshl_b32 s52, s52, 14\n\t" \
    "s_add_u32 s56, s36, s52\n\t" \
    "s_addc_u32 s57, s37, 0\n\t" \
    "s_lshl_b32 s62, s46, 12\n\t" \
    "s_add_u32 s62, s62, %6\n\t" \
    "s_add_u32 s63, s62, 0x18000\n\t" \
    "s_lshl_b32 s60, -1, s44\n\t" \
    "s_bitcmp1_b32 s44, 1\n\t" \
    "s_cselect_b32 s51, 0x33333333, s60\n\t" \
    "s_cselect_b32 s52, s60, 0\n\t" \
    "s_cmp_eq_u32 s52, 0\n\t" \
    "s_cselect_b32 s51, 0xcccccccc, s51\n\t" \
    "s_andn2_b32 s51, s51, s60\n\t" \
    "s_cmp_eq_u32 s45, 0\n\t" \
    "s_cselect_b32 s52, 0, s60\n\t" \
    "s_cselect_b32 s61, -1, s60\n\t" \
    "s_cselect_b32 s73, s60, -1\n\t" \
    "s_or_b32 s51, s51, s52\n\t" \
    "s_and_b32 s61, s61, 0x1ffff\n\t" \
    "s_lshr_b32 s61, s61, 2\n\t" \
    "s_and_b32 s73, s73, 0x1ffff\n\t" \
    "s_lshr_b32 s73, s73, 1\n\t" \
    "s_lshr_b32 s60, s51, 1\n\t" \
    "s_xor_b32 s60, s60, s51\n\t" \
    "s_sub_u32 s52, s44, s45\n\t" \
    "s_add_u32 s52, s52, 1\n\t" \
    "s_lshl_b32 s80, -1, s52\n\t" \
    "s_lshr_b32 s80, s80, 3\n\t" \
    "s_cmp_lt_u32 s46, 2\n\t" \
    "s_cselect_b32 s52, 7, 6\n\t" \
    "s_cselect_b32 s54, 0x10000, 0\n\t" \
    "s_sub_u32 s53, s44, 2\n\t" \
    "s_lshl_b32 s81, s52, s53\n\t" \
    "s_or_b32 s81, s81, s54\n\t" \
    "s_lshl_b32 s82, 2, s53\n\t" \
    "s_lshl_b32 s83, 8, s53\n\t" \
    "s_cmp_eq_u32 s45, 0\n\t" \
    "s_cselect_b32 s81, s81, 0x10000\n\t" \
    "s_cselect_b32 s82, s82, 1\n\t" \
    "s_cselect_b32 s83, s83, 0\n\t" \
    "s_or_b32 s81, s81, 0x20001\n\t" \
    "s_or_b32 s82, s82, 65536\n\t" \
    "s_or_b32 s83, s83, 3\n\t" \
    "s_lshl_b32 s48, 2, s53\n\t" \
    "s_cmp_eq_u32 s45, 0\n\t" \
    "s_cselect_b32 s48, s48, 0x10000\n\t" \
    "s_cmp_lt_u32 s46, 2\n\t" \
    "s_cselect_b32 s48, s48, 0\n\t" \
    "s_lshr_b32 s52, s48, 1\n\t" \
    "s_or_b32 s82, s82, s52\n\t" \
    "s_lshl_b32 s52, s48, 1\n\t" \
    "s_or_b32 s83, s83, s52\n\t" \
    "s_cmp_eq_u32 %5, 0\n\t" \
    "s_cselect_b32 s50, 3, 99\n\t" \
    "s_cselect_b32 s52, 8, 0\n\t" \
    "s_or_b32 s81, s81, s52\n\t" \
    "s_getpc_b64 s[88:89]\n\t" \
    "L_ref%=:\n\t" \
    "s_add_u32 s74, s88, L_mA%=-L_ref%=\n\t" \
    "s_addc_u32 s75, s89, 0\n\t" \
    "s_add_u32 s84, s88, L_mB%=-L_ref%=\n\t" \
    "s_addc_u32 s85, s89, 0\n\t" \
    "s_add_u32 s76, s88, L_mZ%=-L_ref%=\n\t" \
    "s_addc_u32 s77, s89, 0\n\t" \
    "s_add_u32 s86, s88, L_mN%=-L_ref%=\n\t" \
    "s_addc_u32 s87, s89, 0\n\t" \
    "s_add_u32 s90, s88, L_first%=-L_ref%=\n\t" \
    "s_addc_u32 s91, s89, 0\n\t" \
    "s_cmp_eq_u32 s46, 2\n\t" \
    "s_cselect_b64 s[76:77], s[74:75], s[76:77]\n\t" \
    "s_cmp_eq_u32 s46, 3\n\t" \
    "s_cselect_b64 s[76:77], s[84:85], s[76:77]\n\t" \
    "s_cmp_eq_u32 s46, 1\n\t" \
    "s_cselect_b64 s[74:75], s[84:85], s[74:75]\n\t" \
    "s_cmp_gt_u32 s46, 1\n\t" \
    "s_cselect_b64 s[74:75], s[86:87], s[74:75]\n\t" \
    "s_cmp_eq_u32 s45, 0\n\t" \
    "s_cselect_b64 s[84:85], s[74:75], s[76:77]\n\t" \
    "s_add_u32 s88, s88, L_exp%=-L_ref%=\n\t" \
    "s_addc_u32 s89, s89, 0\n\t" \
    "s_mov_b64 s[86:87], s[88:89]\n\t" \
    "v_mbcnt_lo_u32_b32 v248, -1, 0\n\t" \
    "v_mbcnt_hi_u32_b32 v248, -1, v248\n\t" \
    "v_lshlrev_b32 v234, 4, v248\n\t" \
    "s_and_b32 s52, s51, 1\n\t" \
    "s_lshl_b32 s52, s52, 14\n\t" \
    "s_add_u32 s52, s52, %6\n\t" \
    "v_add_u32 v232, s52, v234\n\t" \
    "v_add_u32 v233, 0x10000, v232\n\t" \
    "s_lshl_b32 s52, s46, 13\n\t" \
    "s_add_u32 s52, s52, %6\n\t" \
    "s_add_u32 s53, s52, 0x20000\n\t" \
    "v_add_u32 v235, s53, v234\n\t" \
    "s_lshl_b32 s53, s45, 15\n\t" \
    "s_add_u32 s52, s52, s53\n\t" \
    "s_add_u32 s52, s52, 0x18000\n\t" \
    "v_add_u32 v249, s52, v234\n\t" \
    "v_mov_b32 v238, 0xff800000\n\t" \
    "v_and_b32 v250, 15, v248\n\t" \
    "v_lshrrev_b32 v251, 4, v248\n\t" \
    "v_lshlrev_b32 v251, 2, v251\n\t" \
    "v_sub_u32 v250, v250, v251\n\t" \
    "v_cmp_gt_i32 s[64:65], 0, v250\n\t" \
    "v_mov_b32 v148, 0x3f803f80\n\t" \
    "v_cmp_gt_i32 s[66:67], 1, v250\n\t" \
    "v_mov_b32 v149, 0x3f803f80\n\t" \
    "v_cmp_gt_i32 s[68:69], 2, v250\n\t" \
    "v_mov_b32 v150, 0x3f803f80\n\t" \
    "v_cmp_gt_i32 s[70:71], 3, v250\n\t" \
    "v_mov_b32 v151, 0x3f803f80\n\t" \
    "v_mov_b32 v136, 0\n\t" \
    "v_mov_b32 v137, 0\n\t" \
    "v_mov_b32 v138, 0\n\t" \
    "v_mov_b32 v139, 0\n\t" \
    "v_mov_b32 v140, 0\n\t" \
    "v_mov_b32 v141, 0\n\t" \
    "v_mov_b32 v142, 0\n\t" \
    "v_mov_b32 v143, 0\n\t" \
    "s_waitcnt vmcnt(8) lgkmcnt(0)\n\t" \
    "s_barrier\n\t" \
    "ds_read_b128 v[8:11], v249 offset:0\n\t" \
    "ds_read_b128 v[12:15], v249 offset:1024\n\t" \
    "ds_read_b128 v[16:19], v249 offset:2048\n\t" \
    "ds_read_b128 v[20:23], v249 offset:3072\n\t" \
    "ds_read_b128 v[24:27], v249 offset:4096\n\t" \
    "ds_read_b128 v[28:31], v249 offset:5120\n\t" \
    "ds_read_b128 v[32:35], v249 offset:6144\n\t" \
    "ds_read_b128 v[36:39], v249 offset:7168\n\t" \
    "ds_read_b128 v[168:171], v232 offset:0\n\t" \
    "ds_read_b128 v[172:175], v232 offset:4096\n\t" \
    "ds_read_b128 v[176:179], v232 offset:8192\n\t" \
    "ds_read_b128 v[180:183], v232 offset:12288\n\t" \
    "ds_read_b128 v[184:187], v232 offset:1024\n\t" \
    "ds_read_b128 v[188:191], v232 offset:5120\n\t" \
    "global_load_dword v239, %8, %7\n\t" \
    "s_branch L_qk%=\n\t" \
    "L_loop%=:\n\t" \
    "s_barrier\n\t" \
    "L_qk%=:\n\t" \
    "s_setprio 1\n\t" \
    "s_waitcnt lgkmcnt(4)\n\t" \
    "v_mfma_f32_16x16x32_bf16 v[104:107], v[168:171], v[8:11], v[136:139]\n\t" \
    "ds_read_b128 v[192:195], v232 offset:9216\n\t" \
    "v_mfma_f32_16x16x32_bf16 v[120:123], v[168:171], v[24:27], v[140:143]\n\t" \
    "v_mfma_f32_16x16x32_bf16 v[108:111], v[172:175], v[8:11], v[136:139]\n\t" \
    "ds_read_b128 v[196:199], v232 offset:13312\n\t" \
    "v_mfma_f32_16x16x32_bf16 v[124:127], v[172:175], v[24:27], v[140:143]\n\t" \
    "s_waitcnt lgkmcnt(4)\n\t" \
    "v_mfma_f32_16x16x32_bf16 v[112:115], v[176:179], v[8:11], v[136:139]\n\t" \
    "ds_read_b128 v[168:171], v232 offset:2048\n\t" \
    "v_mfma_f32_16x16x32_bf16 v[128:131], v[176:179], v[24:27], v[140:143]\n\t" \
    "v_mfma_f32_16x16x32_bf16 v[116:119], v[180:183], v[8:11], v[136:139]\n\t" \
    "ds_read_b128 v[172:175], v232 offset:6144\n\t" \
    "v_mfma_f32_16x16x32_bf16 v[132:135], v[180:183], v[24:27], v[140:143]\n\t" \
    "s_waitcnt lgkmcnt(4)\n\t" \
    "v_mfma_f32_16x16x32_bf16 v[104:107], v[184:187], v[12:15], v[104:107]\n\t" \
    "ds_read_b128 v[176:179], v232 offset:10240\n\t" \
    "v_mfma_f32_16x16x32_bf16 v[120:123], v[184:187], v[28:31], v[120:123]\n\t" \
    "v_mfma_f32_16x16x32_bf16 v[108:111], v[188:191], v[12:15], v[108:111]\n\t" \
    "ds_read_b128 v[180:183], v232 offset:14336\n\t" \
    "v_mfma_f32_16x16x32_bf16 v[124:127], v[188:191], v[28:31], v[124:127]\n\t" \
    "s_waitcnt lgkmcnt(4)\n\t" \
    "v_mfma_f32_16x16x32_bf16 v[112:115], v[192:195], v[12:15], v[112:115]\n\t" \
    "ds_read_b128 v[184:187], v232 offset:3072\n\t" \
    "v_mfma_f32_16x16x32_bf16 v[128:131], v[192:195], v[28:31], v[128:131]\n\t" \
    "v_mfma_f32_16x16x32_bf16 v[116:119], v[196:199], v[12:15], v[116:119]\n\t" \
    "ds_read_b128 v[188:191], v232 offset:7168\n\t" \
    "v_mfma_f32_16x16x32_bf16 v[132:135], v[196:199], v[28:31], v[132:135]\n\t" \
    "s_waitcnt lgkmcnt(4)\n\t" \
    "v_mfma_f32_16x16x32_bf16 v[104:107], v[168:171], v[16:19], v[104:107]\n\t" \
    "ds_read_b128 v[192:195], v232 offset:11264\n\t" \
    "v_mfma_f32_16x16x32_bf16 v[120:123], v[168:171], v[32:35], v[120:123]\n\t" \
    "v_mfma_f32_16x16x32_bf16 v[108:111], v[172:175], v[16:19], v[108:111]\n\t" \
    "ds_read_b128 v[196:199], v232 offset:15360\n\t" \
    "v_mfma_f32_16x16x32_bf16 v[124:127], v[172:175], v[32:35], v[124:127]\n\t" \
    "s_waitcnt lgkmcnt(4)\n\t" \
    "v_mfma_f32_16x16x32_bf16 v[112:115], v[176:179], v[16:19], v[112:115]\n\t" \
    "ds_read_b128 v[168:171], v233 offset:32768\n\t" \
    "v_mfma_f32_16x16x32_bf16 v[128:131], v[176:179], v[32:35], v[128:131]\n\t" \
    "v_mfma_f32_16x16x32_bf16 v[116:119], v[180:183], v[16:19], v[116:119]\n\t" \
    "ds_read_b128 v[172:175], v233 offset:33792\n\t" \
    "v_mfma_f32_16x16x32_bf16 v[132:135], v[180:183], v[32:35], v[132:135]\n\t" \
    "s_waitcnt lgkmcnt(4)\n\t" \
    "v_mfma_f32_16x16x32_bf16 v[104:107], v[184:187], v[20:23], v[104:107]\n\t" \
    "ds_read_b128 v[176:179], v233 offset:34816\n\t" \
    "v_mfma_f32_16x16x32_bf16 v[120:123], v[184:187], v[36:39], v[120:123]\n\t" \
    "v_mfma_f32_16x16x32_bf16 v[108:111], v[188:191], v[20:23], v[108:111]\n\t" \
    "ds_read_b128 v[180:183], v233 offset:35840\n\t" \
    "v_mfma_f32_16x16x32_bf16 v[124:127], v[188:191], v[36:39], v[124:127]\n\t" \
    "s_waitcnt lgkmcnt(4)\n\t" \
    "v_mfma_f32_16x16x32_bf16 v[112:115], v[192:195], v[20:23], v[112:115]\n\t" \
    "ds_read_b128 v[184:187], v233 offset:36864\n\t" \
    "v_mfma_f32_16x16x32_bf16 v[128:131], v[192:195], v[36:39], v[128:131]\n\t" \
    "s_bitcmp1_b32 s83, s47\n\t" \
    "v_mfma_f32_16x16x32_bf16 v[116:119], v[196:199], v[20:23], v[116:119]\n\t" \
    "ds_read_b128 v[188:191], v233 offset:37888\n\t" \
    "v_mfma_f32_16x16x32_bf16 v[132:135], v[196:199], v[36:39], v[132:135]\n\t" \
    "s_cbranch_scc1 L_pvz%=\n\t" \
    "L_pv%=:\n\t" \
    "v_mfma_f32_16x16x32_bf16 v[240:243], v[148:151], v[152:155], v[240:243]\n\t" \
    "v_mfma_f32_16x16x32_bf16 v[244:247], v[148:151], v[160:163], v[244:247]\n\t" \
    "s_waitcnt lgkmcnt(4)\n\t" \
    "v_mfma_f32_16x16x32_bf16 v[40:43], v[168:171], v[152:155], v[40:43]\n\t" \
    "ds_read_b128 v[192:195], v233 offset:38912\n\t" \
    "v_mfma_f32_16x16x32_bf16 v[72:75], v[168:171], v[160:163], v[72:75]\n\t" \
    "v_mfma_f32_16x16x32_bf16 v[44:47], v[172:175], v[152:155], v[44:47]\n\t" \
    "ds_read_b128 v[196:199], v233 offset:39936\n\t" \
    "v_mfma_f32_16x16x32_bf16 v[76:79], v[172:175], v[160:163], v[76:79]\n\t" \
    "s_waitcnt lgkmcnt(4)\n\t" \
    "v_mfma_f32_16x16x32_bf16 v[48:51], v[176:179], v[152:155], v[48:51]\n\t" \
    "ds_read_b128 v[168:171], v233 offset:40960\n\t" \
    "v_mfma_f32_16x16x32_bf16 v[80:83], v[176:179], v[160:163], v[80:83]\n\t" \
    "v_mfma_f32_16x16x32_bf16 v[52:55], v[180:183], v[152:155], v[52:55]\n\t" \
    "ds_read_b128 v[172:175], v233 offset:41984\n\t" \
    "v_mfma_f32_16x16x32_bf16 v[84:87], v[180:183], v[160:163], v[84:87]\n\t" \
    "s_waitcnt lgkmcnt(4)\n\t" \
    "v_mfma_f32_16x16x32_bf16 v[56:59], v[184:187], v[152:155], v[56:59]\n\t" \
    "ds_read_b128 v[176:179], v233 offset:43008\n\t" \
    "v_mfma_f32_16x16x32_bf16 v[88:91], v[184:187], v[160:163], v[88:91]\n\t" \
    "v_mfma_f32_16x16x32_bf16 v[60:63], v[188:191], v[152:155], v[60:63]\n\t" \
    "ds_read_b128 v[180:183], v233 offset:44032\n\t" \
    "v_mfma_f32_16x16x32_bf16 v[92:95], v[188:191], v[160:163], v[92:95]\n\t" \
    "s_waitcnt lgkmcnt(4)\n\t" \
    "v_mfma_f32_16x16x32_bf16 v[64:67], v[192:195], v[152:155], v[64:67]\n\t" \
    "ds_read_b128 v[184:187], v233 offset:45056\n\t" \
    "v_mfma_f32_16x16x32_bf16 v[96:99], v[192:195], v[160:163], v[96:99]\n\t" \
    "v_mfma_f32_16x16x32_bf16 v[68:71], v[196:199], v[152:155], v[68:71]\n\t" \
    "ds_read_b128 v[188:191], v233 offset:46080\n\t" \
    "v_mfma_f32_16x16x32_bf16 v[100:103], v[196:199], v[160:163], v[100:103]\n\t" \
    "L_pvrest%=:\n\t" \
    "v_mfma_f32_16x16x32_bf16 v[240:243], v[148:151], v[156:159], v[240:243]\n\t" \
    "v_mfma_f32_16x16x32_bf16 v[244:247], v[148:151], v[164:167], v[244:247]\n\t" \
    "s_waitcnt lgkmcnt(4)\n\t" \
    "v_mfma_f32_16x16x32_bf16 v[40:43], v[168:171], v[156:159], v[40:43]\n\t" \
    "ds_read_b128 v[192:195], v233 offset:47104\n\t" \
    "v_mfma_f32_16x16x32_bf16 v[72:75], v[168:171], v[164:167], v[72:75]\n\t" \
    "v_mfma_f32_16x16x32_bf16 v[44:47], v[172:175], v[156:159], v[44:47]\n\t" \
    "ds_read_b128 v[196:199], v233 offset:48128\n\t" \
    "v_mfma_f32_16x16x32_bf16 v[76:79], v[172:175], v[164:167], v[76:79]\n\t" \
    "s_waitcnt lgkmcnt(4)\n\t" \
    "v_mfma_f32_16x16x32_bf16 v[48:51], v[176:179], v[156:159], v[48:51]\n\t" \
    "v_mfma_f32_16x16x32_bf16 v[80:83], v[176:179], v[164:167], v[80:83]\n\t" \
    "v_mfma_f32_16x16x32_bf16 v[52:55], v[180:183], v[156:159], v[52:55]\n\t" \
    "v_mfma_f32_16x16x32_bf16 v[84:87], v[180:183], v[164:167], v[84:87]\n\t" \
    "s_setprio 3\n\t" \
    "s_waitcnt vmcnt(0) lgkmcnt(0)\n\t" \
    "s_barrier\n\t" \
    "v_mfma_f32_16x16x32_bf16 v[56:59], v[184:187], v[156:159], v[56:59]\n\t" \
    "v_mfma_f32_16x16x32_bf16 v[88:91], v[184:187], v[164:167], v[88:91]\n\t" \
    "v_mfma_f32_16x16x32_bf16 v[60:63], v[188:191], v[156:159], v[60:63]\n\t" \
    "v_mfma_f32_16x16x32_bf16 v[92:95], v[188:191], v[164:167], v[92:95]\n\t" \
    "s_mov_b32 s93, s92\n\t" \
    "s_mov_b32 s92, 0x10000\n\t" \
    "s_bitcmp1_b32 s60, s47\n\t" \
    "s_cselect_b32 s92, 0x14000, s92\n\t" \
    "v_mfma_f32_16x16x32_bf16 v[64:67], v[192:195], v[156:159], v[64:67]\n\t" \
    "v_mfma_f32_16x16x32_bf16 v[96:99], v[192:195], v[164:167], v[96:99]\n\t" \
    "s_bitcmp1_b32 s81, s47\n\t" \
    "v_mfma_f32_16x16x32_bf16 v[68:71], v[196:199], v[156:159], v[68:71]\n\t" \
    "v_mfma_f32_16x16x32_bf16 v[100:103], v[196:199], v[164:167], v[100:103]\n\t" \
    "s_setprio 0\n\t" \
    "L_v%=:\n\t" \
    "v_xor_b32 v232, s92, v232\n\t" \
    "v_xor_b32 v233, s93, v233\n\t" \
    "s_cbranch_scc1 L_special%=\n\t" \
    "L_exp%=:\n\t" \
    "s_add_u32 s53, s47, 2\n\t" \
    "s_bitcmp1_b32 s51, s53\n\t" \
    "s_cselect_b32 s53, 0x4000, 0\n\t" \
    "s_add_u32 m0, s53, s62\n\t" \
    "s_bitcmp1_b32 s61, s47\n\t" \
    "s_cbranch_scc0 L_sk0%=\n\t" \
    "global_load_lds_dwordx4 v234, s[56:57] offset:0 sc1\n\t" \
    "L_sk0%=:\n\t" \
    "v_exp_f32 v200, v104\n\t" \
    "v_exp_f32 v216, v120\n\t" \
    "ds_read_b128 v[168:171], v232 offset:0\n\t" \
    "v_exp_f32 v201, v105\n\t" \
    "s_cbranch_scc0 L_sk1%=\n\t" \
    "global_load_lds_dwordx4 v234, s[56:57] offset:1024 sc1\n\t" \
    "L_sk1%=:\n\t" \
    "v_exp_f32 v217, v121\n\t" \
    "v_exp_f32 v202, v106\n\t" \
    "ds_read_b128 v[172:175], v232 offset:4096\n\t" \
    "v_exp_f32 v218, v122\n\t" \
    "s_cbranch_scc0 L_sk2%=\n\t" \
    "global_load_lds_dwordx4 v234, s[56:57] offset:2048 sc1\n\t" \
    "L_sk2%=:\n\t" \
    "v_exp_f32 v203, v107\n\t" \
    "v_cvt_pk_bf16_f32 v152, v200, v201\n\t" \
    "v_cvt_pk_bf16_f32 v160, v216, v217\n\t" \
    "v_exp_f32 v219, v123\n\t" \
    "ds_read_b128 v[176:179], v232 offset:8192\n\t" \
    "v_exp_f32 v204, v108\n\t" \
    "s_cbranch_scc0 L_sk3%=\n\t" \
    "global_load_lds_dwordx4 v234, s[56:57] offset:3072 sc1\n\t" \
    "L_sk3%=:\n\t" \
    "v_exp_f32 v220, v124\n\t" \
    "v_exp_f32 v205, v109\n\t" \
    "ds_read_b128 v[180:183], v232 offset:12288\n\t" \
    "v_cvt_pk_bf16_f32 v153, v202, v203\n\t" \
    "v_cvt_pk_bf16_f32 v161, v218, v219\n\t" \
    "v_exp_f32 v221, v125\n\t" \
    "s_add_u32 s53, s47, 1\n\t" \
    "s_bitcmp1_b32 s51, s53\n\t" \
    "s_cselect_b32 s53, 0x4000, 0\n\t" \
    "s_add_u32 m0, s53, s63\n\t" \
    "s_bitcmp1_b32 s73, s47\n\t" \
    "s_cbranch_scc0 L_sv0%=\n\t" \
    "global_load_lds_dwordx4 v234, s[58:59] offset:0 sc1\n\t" \
    "L_sv0%=:\n\t" \
    "v_exp_f32 v206, v110\n\t" \
    "v_exp_f32 v222, v126\n\t" \
    "ds_read_b128 v[184:187], v232 offset:1024\n\t" \
    "v_exp_f32 v207, v111\n\t" \
    "v_cvt_pk_bf16_f32 v154, v204, v205\n\t" \
    "v_cvt_pk_bf16_f32 v162, v220, v221\n\t" \
    "s_cbranch_scc0 L_sv1%=\n\t" \
    "global_load_lds_dwordx4 v234, s[58:59] offset:1024 sc1\n\t" \
    "L_sv1%=:\n\t" \
    "v_exp_f32 v223, v127\n\t" \
    "v_exp_f32 v208, v112\n\t" \
    "ds_read_b128 v[188:191], v232 offset:5120\n\t" \
    "v_or_b32 v248, v152, v160\n\t" \
    "v_or3_b32 v248, v248, v153, v161\n\t" \
    "v_exp_f32 v224, v128\n\t" \
    "s_cbranch_scc0 L_sv2%=\n\t" \
    "global_load_lds_dwordx4 v234, s[58:59] offset:2048 sc1\n\t" \
    "L_sv2%=:\n\t" \
    "v_exp_f32 v209, v113\n\t" \
    "v_cvt_pk_bf16_f32 v155, v206, v207\n\t" \
    "v_cvt_pk_bf16_f32 v163, v222, v223\n\t" \
    "v_exp_f32 v225, v129\n\t" \
    "v_exp_f32 v210, v114\n\t" \
    "s_cbranch_scc0 L_sv3%=\n\t" \
    "global_load_lds_dwordx4 v234, s[58:59] offset:3072 sc1\n\t" \
    "L_sv3%=:\n\t" \
    "v_exp_f32 v226, v130\n\t" \
    "v_exp_f32 v211, v115\n\t" \
    "v_cvt_pk_bf16_f32 v156, v208, v209\n\t" \
    "v_cvt_pk_bf16_f32 v164, v224, v225\n\t" \
    "v_exp_f32 v227, v131\n\t" \
    "s_add_u32 s58, s56, s78\n\t" \
    "s_addc_u32 s59, s57, s79\n\t" \
    "v_exp_f32 v212, v116\n\t" \
    "s_mov_b32 s53, 0x4000\n\t" \
    "s_bitcmp1_b32 s80, s47\n\t" \
    "s_cselect_b32 s52, 0x8000, s53\n\t" \
    "v_or3_b32 v248, v248, v154, v162\n\t" \
    "v_or3_b32 v248, v248, v155, v163\n\t" \
    "v_exp_f32 v228, v132\n\t" \
    "s_add_u32 s56, s56, s52\n\t" \
    "s_addc_u32 s57, s57, 0\n\t" \
    "v_exp_f32 v213, v117\n\t" \
    "s_xor_b32 s62, s62, 0x10000\n\t" \
    "v_cvt_pk_bf16_f32 v157, v210, v211\n\t" \
    "v_cvt_pk_bf16_f32 v165, v226, v227\n\t" \
    "v_exp_f32 v229, v133\n\t" \
    "s_xor_b32 s63, s63, 0x10000\n\t" \
    "v_exp_f32 v214, v118\n\t" \
    "v_exp_f32 v230, v134\n\t" \
    "v_exp_f32 v215, v119\n\t" \
    "v_cvt_pk_bf16_f32 v158, v212, v213\n\t" \
    "v_cvt_pk_bf16_f32 v166, v228, v229\n\t" \
    "v_exp_f32 v231, v135\n\t" \
    "v_cvt_pk_bf16_f32 v159, v214, v215\n\t" \
    "v_or3_b32 v248, v248, v156, v164\n\t" \
    "v_cvt_pk_bf16_f32 v167, v230, v231\n\t" \
    "v_or3_b32 v248, v248, v157, v165\n\t" \
    "v_or3_b32 v248, v248, v158, v166\n\t" \
    "v_or3_b32 v248, v248, v159, v167\n\t" \
    "v_and_b32 v248, 0x40004000, v248\n\t" \
    "v_cmp_ne_u32 vcc, 0, v248\n\t" \
    "s_cbranch_vccnz L_rare%=\n\t" \
    "L_sumdone%=:\n\t" \
    "s_bitcmp1_b32 s82, s47\n\t" \
    "s_cbranch_scc1 L_post%=\n\t" \
    "L_vend%=:\n\t" \
    "s_add_u32 s47, s47, 1\n\t" \
    "s_branch L_loop%=\n\t" \
    "L_mend0%=:\n\t" \
    "s_mov_b32 s92, 0x10000\n\t" \
    "s_mov_b32 s93, s92\n\t" \
    "s_mov_b32 s92, 0x10000\n\t" \
    "s_bitcmp1_b32 s60, s47\n\t" \
    "s_cselect_b32 s92, 0x14000, s92\n\t" \
    "s_setprio 3\n\t" \
    "s_waitcnt vmcnt(1) lgkmcnt(0)\n\t" \
    "s_barrier\n\t" \
    "s_nop 7\n\t" \
    "s_setprio 0\n\t" \
    "s_cmp_eq_u32 s47, 0\n\t" \
    "s_branch L_v%=\n\t" \
    "L_pvz%=:\n\t" \
    "s_cmp_eq_u32 s47, 0\n\t" \
    "s_cbranch_scc1 L_mend0%=\n\t" \
    "s_sub_u32 s52, s47, 1\n\t" \
    "s_bitcmp1_b32 s48, s52\n\t" \
    "s_cbranch_scc0 L_pvz2%=\n\t" \
    "s_mov_b32 s93, s92\n\t" \
    "s_mov_b32 s92, 0x10000\n\t" \
    "s_bitcmp1_b32 s60, s47\n\t" \
    "s_cselect_b32 s92, 0x14000, s92\n\t" \
    "s_setprio 3\n\t" \
    "s_waitcnt vmcnt(0) lgkmcnt(0)\n\t" \
    "s_barrier\n\t" \
    "s_nop 7\n\t" \
    "s_setprio 0\n\t" \
    "s_bitcmp1_b32 s81, s47\n\t" \
    "s_branch L_v%=\n\t" \
    "L_pvz2%=:\n\t" \
    "v_mfma_f32_16x16x32_bf16 v[240:243], v[148:151], v[152:155], 0\n\t" \
    "v_mfma_f32_16x16x32_bf16 v[244:247], v[148:151], v[160:163], 0\n\t" \
    "s_waitcnt lgkmcnt(4)\n\t" \
    "v_mfma_f32_16x16x32_bf16 v[40:43], v[168:171], v[152:155], 0\n\t" \
    "ds_read_b128 v[192:195], v233 offset:38912\n\t" \
    "v_mfma_f32_16x16x32_bf16 v[72:75], v[168:171], v[160:163], 0\n\t" \
    "v_mfma_f32_16x16x32_bf16 v[44:47], v[172:175], v[152:155], 0\n\t" \
    "ds_read_b128 v[196:199], v233 offset:39936\n\t" \
    "v_mfma_f32_16x16x32_bf16 v[76:79], v[172:175], v[160:163], 0\n\t" \
    "s_waitcnt lgkmcnt(4)\n\t" \
    "v_mfma_f32_16x16x32_bf16 v[48:51], v[176:179], v[152:155], 0\n\t" \
    "ds_read_b128 v[168:171], v233 offset:40960\n\t" \
    "v_mfma_f32_16x16x32_bf16 v[80:83], v[176:179], v[160:163], 0\n\t" \
    "v_mfma_f32_16x16x32_bf16 v[52:55], v[180:183], v[152:155], 0\n\t" \
    "ds_read_b128 v[172:175], v233 offset:41984\n\t" \
    "v_mfma_f32_16x16x32_bf16 v[84:87], v[180:183], v[160:163], 0\n\t" \
    "s_waitcnt lgkmcnt(4)\n\t" \
    "v_mfma_f32_16x16x32_bf16 v[56:59], v[184:187], v[152:155], 0\n\t" \
    "ds_read_b128 v[176:179], v233 offset:43008\n\t" \
    "v_mfma_f32_16x16x32_bf16 v[88:91], v[184:187], v[160:163], 0\n\t" \
    "v_mfma_f32_16x16x32_bf16 v[60:63], v[188:191], v[152:155], 0\n\t" \
    "ds_read_b128 v[180:183], v233 offset:44032\n\t" \
    "v_mfma_f32_16x16x32_bf16 v[92:95], v[188:191], v[160:163], 0\n\t" \
    "s_waitcnt lgkmcnt(4)\n\t" \
    "v_mfma_f32_16x16x32_bf16 v[64:67], v[192:195], v[152:155], 0\n\t" \
    "ds_read_b128 v[184:187], v233 offset:45056\n\t" \
    "v_mfma_f32_16x16x32_bf16 v[96:99], v[192:195], v[160:163], 0\n\t" \
    "v_mfma_f32_16x16x32_bf16 v[68:71], v[196:199], v[152:155], 0\n\t" \
    "ds_read_b128 v[188:191], v233 offset:46080\n\t" \
    "v_mfma_f32_16x16x32_bf16 v[100:103], v[196:199], v[160:163], 0\n\t" \
    "s_branch L_pvrest%=\n\t" \
    "L_special%=:\n\t" \
    "s_setprio 2\n\t" \
    "s_cmp_eq_u32 s47, s50\n\t" \
    "s_cbranch_scc1 L_pub%=\n\t" \
    "L_disp%=:\n\t" \
    "s_cmp_eq_u32 s47, 17\n\t" \
    "s_cbranch_scc1 L_final%=\n\t" \
    "s_cmp_eq_u32 s47, s72\n\t" \
    "s_cbranch_scc1 L_seam%=\n\t" \
    "s_cmp_eq_u32 s47, 0\n\t" \
    "s_cbranch_scc1 L_zero%=\n\t" \
    "s_add_u32 s52, s47, 1\n\t" \
    "s_cmp_eq_u32 s52, s72\n\t" \
    "s_cselect_b64 s[54:55], s[76:77], s[84:85]\n\t" \
    "s_setpc_b64 s[54:55]\n\t" \
    "L_mA%=:\n\t" \
    "v_cndmask_b32 v104, v104, v238, s[64:65]\n\t" \
    "v_cndmask_b32 v105, v105, v238, s[66:67]\n\t" \
    "v_cndmask_b32 v106, v106, v238, s[68:69]\n\t" \
    "v_cndmask_b32 v107, v107, v238, s[70:71]\n\t" \
    "v_mov_b32 v108, v238\n\t" \
    "v_mov_b32 v109, v238\n\t" \
    "v_mov_b32 v110, v238\n\t" \
    "v_mov_b32 v111, v238\n\t" \
    "v_mov_b32 v112, v238\n\t" \
    "v_mov_b32 v113, v238\n\t" \
    "v_mov_b32 v114, v238\n\t" \
    "v_mov_b32 v115, v238\n\t" \
    "v_mov_b32 v116, v238\n\t" \
    "v_mov_b32 v117, v238\n\t" \
    "v_mov_b32 v118, v238\n\t" \
    "v_mov_b32 v119, v238\n\t" \
    "v_cndmask_b32 v124, v124, v238, s[64:65]\n\t" \
    "v_cndmask_b32 v125, v125, v238, s[66:67]\n\t" \
    "v_cndmask_b32 v126, v126, v238, s[68:69]\n\t" \
    "v_cndmask_b32 v127, v127, v238, s[70:71]\n\t" \
    "v_mov_b32 v128, v238\n\t" \
    "v_mov_b32 v129, v238\n\t" \
    "v_mov_b32 v130, v238\n\t" \
    "v_mov_b32 v131, v238\n\t" \
    "v_mov_b32 v132, v238\n\t" \
    "v_mov_b32 v133, v238\n\t" \
    "v_mov_b32 v134, v238\n\t" \
    "v_mov_b32 v135, v238\n\t" \
    "s_setpc_b64 s[86:87]\n\t" \
    "L_mB%=:\n\t" \
    "v_cndmask_b32 v112, v112, v238, s[64:65]\n\t" \
    "v_cndmask_b32 v113, v113, v238, s[66:67]\n\t" \
    "v_cndmask_b32 v114, v114, v238, s[68:69]\n\t" \
    "v_cndmask_b32 v115, v115, v238, s[70:71]\n\t" \
    "v_mov_b32 v116, v238\n\t" \
    "v_mov_b32 v117, v238\n\t" \
    "v_mov_b32 v118, v238\n\t" \
    "v_mov_b32 v119, v238\n\t" \
    "v_cndmask_b32 v132, v132, v238, s[64:65]\n\t" \
    "v_cndmask_b32 v133, v133, v238, s[66:67]\n\t" \
    "v_cndmask_b32 v134, v134, v238, s[68:69]\n\t" \
    "v_cndmask_b32 v135, v135, v238, s[70:71]\n\t" \
    "s_setpc_b64 s[86:87]\n\t" \
    "L_mZ%=:\n\t" \
    "v_mov_b32 v104, v238\n\t" \
    "v_mov_b32 v105, v238\n\t" \
    "v_mov_b32 v106, v238\n\t" \
    "v_mov_b32 v107, v238\n\t" \
    "v_mov_b32 v108, v238\n\t" \
    "v_mov_b32 v109, v238\n\t" \
    "v_mov_b32 v110, v238\n\t" \
    "v_mov_b32 v111, v238\n\t" \
    "v_mov_b32 v112, v238\n\t" \
    "v_mov_b32 v113, v238\n\t" \
    "v_mov_b32 v114, v238\n\t" \
    "v_mov_b32 v115, v238\n\t" \
    "v_mov_b32 v116, v238\n\t" \
    "v_mov_b32 v117, v238\n\t" \
    "v_mov_b32 v118, v238\n\t" \
    "v_mov_b32 v119, v238\n\t" \
    "v_mov_b32 v120, v238\n\t" \
    "v_mov_b32 v121, v238\n\t" \
    "v_mov_b32 v122, v238\n\t" \
    "v_mov_b32 v123, v238\n\t" \
    "v_mov_b32 v124, v238\n\t" \
    "v_mov_b32 v125, v238\n\t" \
    "v_mov_b32 v126, v238\n\t" \
    "v_mov_b32 v127, v238\n\t" \
    "v_mov_b32 v128, v238\n\t" \
    "v_mov_b32 v129, v238\n\t" \
    "v_mov_b32 v130, v238\n\t" \
    "v_mov_b32 v131, v238\n\t" \
    "v_mov_b32 v132, v238\n\t" \
    "v_mov_b32 v133, v238\n\t" \
    "v_mov_b32 v134, v238\n\t" \
    "v_mov_b32 v135, v238\n\t" \
    "s_setpc_b64 s[86:87]\n\t" \
    "L_mN%=:\n\t" \
    "s_setpc_b64 s[86:87]\n\t" \
    "L_pub%=:\n\t" \
    "v_mov_b32 v248, %10\n\t" \
    "v_mov_b32 v249, %11\n\t" \
    "v_mov_b32 v250, 0\n\t" \
    "s_mov_b64 exec, 1\n\t" \
    "global_store_dwordx2 v250, v[248:249], %9 sc1\n\t" \
    "s_mov_b64 exec, -1\n\t" \
    "s_cmp_eq_u32 s44, 4\n\t" \
    "s_cbranch_scc0 L_exp%=\n\t" \
    "s_branch L_disp%=\n\t" \
    "L_zero%=:\n\t" \
    "s_cmp_eq_u32 s72, 2\n\t" \
    "s_cbranch_scc0 L_first%=\n\t" \
    "L_maskfirst%=:\n\t" \
    "s_mov_b64 s[86:87], s[90:91]\n\t" \
    "s_setpc_b64 s[74:75]\n\t" \
    "L_seam%=:\n\t" \
    "s_nop 7\n\t" \
    "v_rcp_f32 v248, v240\n\t" \
    "v_rcp_f32 v250, v244\n\t" \
    "s_nop 0\n\t" \
    "v_add_u32 v255, 180224, v234\n\t" \
    "v_mul_f32 v40, v40, v248\n\t" \
    "v_mul_f32 v41, v41, v248\n\t" \
    "v_mul_f32 v42, v42, v248\n\t" \
    "v_mul_f32 v43, v43, v248\n\t" \
    "v_mul_f32 v44, v44, v248\n\t" \
    "v_mul_f32 v45, v45, v248\n\t" \
    "v_mul_f32 v46, v46, v248\n\t" \
    "v_mul_f32 v47, v47, v248\n\t" \
    "v_cvt_pk_bf16_f32 v200, v40, v41\n\t" \
    "v_cvt_pk_bf16_f32 v201, v42, v43\n\t" \
    "v_cvt_pk_bf16_f32 v202, v44, v45\n\t" \
    "v_cvt_pk_bf16_f32 v203, v46, v47\n\t" \
    "global_store_dwordx4 v234, v[200:203], s[40:41] offset:0 sc1\n\t" \
    "v_mul_f32 v48, v48, v248\n\t" \
    "v_mul_f32 v49, v49, v248\n\t" \
    "v_mul_f32 v50, v50, v248\n\t" \
    "v_mul_f32 v51, v51, v248\n\t" \
    "v_mul_f32 v52, v52, v248\n\t" \
    "v_mul_f32 v53, v53, v248\n\t" \
    "v_mul_f32 v54, v54, v248\n\t" \
    "v_mul_f32 v55, v55, v248\n\t" \
    "v_cvt_pk_bf16_f32 v204, v48, v49\n\t" \
    "v_cvt_pk_bf16_f32 v205, v50, v51\n\t" \
    "v_cvt_pk_bf16_f32 v206, v52, v53\n\t" \
    "v_cvt_pk_bf16_f32 v207, v54, v55\n\t" \
    "global_store_dwordx4 v234, v[204:207], s[40:41] offset:1024 sc1\n\t" \
    "v_mul_f32 v56, v56, v248\n\t" \
    "v_mul_f32 v57, v57, v248\n\t" \
    "v_mul_f32 v58, v58, v248\n\t" \
    "v_mul_f32 v59, v59, v248\n\t" \
    "v_mul_f32 v60, v60, v248\n\t" \
    "v_mul_f32 v61, v61, v248\n\t" \
    "v_mul_f32 v62, v62, v248\n\t" \
    "v_mul_f32 v63, v63, v248\n\t" \
    "v_cvt_pk_bf16_f32 v208, v56, v57\n\t" \
    "v_cvt_pk_bf16_f32 v209, v58, v59\n\t" \
    "v_cvt_pk_bf16_f32 v210, v60, v61\n\t" \
    "v_cvt_pk_bf16_f32 v211, v62, v63\n\t" \
    "global_store_dwordx4 v234, v[208:211], s[40:41] offset:2048 sc1\n\t" \
    "v_mul_f32 v64, v64, v248\n\t" \
    "v_mul_f32 v65, v65, v248\n\t" \
    "v_mul_f32 v66, v66, v248\n\t" \
    "v_mul_f32 v67, v67, v248\n\t" \
    "v_mul_f32 v68, v68, v248\n\t" \
    "v_mul_f32 v69, v69, v248\n\t" \
    "v_mul_f32 v70, v70, v248\n\t" \
    "v_mul_f32 v71, v71, v248\n\t" \
    "v_cvt_pk_bf16_f32 v212, v64, v65\n\t" \
    "v_cvt_pk_bf16_f32 v213, v66, v67\n\t" \
    "v_cvt_pk_bf16_f32 v214, v68, v69\n\t" \
    "v_cvt_pk_bf16_f32 v215, v70, v71\n\t" \
    "global_store_dwordx4 v234, v[212:215], s[40:41] offset:3072 sc1\n\t" \
    "v_mul_f32 v72, v72, v250\n\t" \
    "v_mul_f32 v73, v73, v250\n\t" \
    "v_mul_f32 v74, v74, v250\n\t" \
    "v_mul_f32 v75, v75, v250\n\t" \
    "v_mul_f32 v76, v76, v250\n\t" \
    "v_mul_f32 v77, v77, v250\n\t" \
    "v_mul_f32 v78, v78, v250\n\t" \
    "v_mul_f32 v79, v79, v250\n\t" \
    "v_cvt_pk_bf16_f32 v216, v72, v73\n\t" \
    "v_cvt_pk_bf16_f32 v217, v74, v75\n\t" \
    "v_cvt_pk_bf16_f32 v218, v76, v77\n\t" \
    "v_cvt_pk_bf16_f32 v219, v78, v79\n\t" \
    "global_store_dwordx4 v255, v[216:219], s[40:41] offset:0 sc1\n\t" \
    "v_mul_f32 v80, v80, v250\n\t" \
    "v_mul_f32 v81, v81, v250\n\t" \
    "v_mul_f32 v82, v82, v250\n\t" \
    "v_mul_f32 v83, v83, v250\n\t" \
    "v_mul_f32 v84, v84, v250\n\t" \
    "v_mul_f32 v85, v85, v250\n\t" \
    "v_mul_f32 v86, v86, v250\n\t" \
    "v_mul_f32 v87, v87, v250\n\t" \
    "v_cvt_pk_bf16_f32 v220, v80, v81\n\t" \
    "v_cvt_pk_bf16_f32 v221, v82, v83\n\t" \
    "v_cvt_pk_bf16_f32 v222, v84, v85\n\t" \
    "v_cvt_pk_bf16_f32 v223, v86, v87\n\t" \
    "global_store_dwordx4 v255, v[220:223], s[40:41] offset:1024 sc1\n\t" \
    "v_mul_f32 v88, v88, v250\n\t" \
    "v_mul_f32 v89, v89, v250\n\t" \
    "v_mul_f32 v90, v90, v250\n\t" \
    "v_mul_f32 v91, v91, v250\n\t" \
    "v_mul_f32 v92, v92, v250\n\t" \
    "v_mul_f32 v93, v93, v250\n\t" \
    "v_mul_f32 v94, v94, v250\n\t" \
    "v_mul_f32 v95, v95, v250\n\t" \
    "v_cvt_pk_bf16_f32 v224, v88, v89\n\t" \
    "v_cvt_pk_bf16_f32 v225, v90, v91\n\t" \
    "v_cvt_pk_bf16_f32 v226, v92, v93\n\t" \
    "v_cvt_pk_bf16_f32 v227, v94, v95\n\t" \
    "global_store_dwordx4 v255, v[224:227], s[40:41] offset:2048 sc1\n\t" \
    "v_mul_f32 v96, v96, v250\n\t" \
    "v_mul_f32 v97, v97, v250\n\t" \
    "v_mul_f32 v98, v98, v250\n\t" \
    "v_mul_f32 v99, v99, v250\n\t" \
    "v_mul_f32 v100, v100, v250\n\t" \
    "v_mul_f32 v101, v101, v250\n\t" \
    "v_mul_f32 v102, v102, v250\n\t" \
    "v_mul_f32 v103, v103, v250\n\t" \
    "v_cvt_pk_bf16_f32 v228, v96, v97\n\t" \
    "v_cvt_pk_bf16_f32 v229, v98, v99\n\t" \
    "v_cvt_pk_bf16_f32 v230, v100, v101\n\t" \
    "v_cvt_pk_bf16_f32 v231, v102, v103\n\t" \
    "global_store_dwordx4 v255, v[228:231], s[40:41] offset:3072 sc1\n\t" \
    "s_nop 1\n\t" \
    "s_cmp_eq_u32 s47, 16\n\t" \
    "s_cbranch_scc1 L_maskfirst%=\n\t" \
    "L_first%=:\n\t" \
    "s_mov_b64 s[86:87], s[88:89]\n\t" \
    "v_max3_f32 v248, v104, v105, v106\n\t" \
    "v_max3_f32 v249, v120, v121, v122\n\t" \
    "v_max3_f32 v248, v248, v107, v108\n\t" \
    "v_max3_f32 v249, v249, v123, v124\n\t" \
    "v_max3_f32 v248, v248, v109, v110\n\t" \
    "v_max3_f32 v249, v249, v125, v126\n\t" \
    "v_max3_f32 v248, v248, v111, v112\n\t" \
    "v_max3_f32 v249, v249, v127, v128\n\t" \
    "v_max3_f32 v248, v248, v113, v114\n\t" \
    "v_max3_f32 v249, v249, v129, v130\n\t" \
    "v_max3_f32 v248, v248, v115, v116\n\t" \
    "v_max3_f32 v249, v249, v131, v132\n\t" \
    "v_max3_f32 v248, v248, v117, v118\n\t" \
    "v_max3_f32 v249, v249, v133, v134\n\t" \
    "v_max_f32 v248, v248, v119\n\t" \
    "v_max_f32 v249, v249, v135\n\t" \
    "v_mov_b32 v250, v248\n\t" \
    "v_mov_b32 v251, v249\n\t" \
    "s_nop 1\n\t" \
    "v_permlane16_swap_b32 v248, v250\n\t" \
    "v_permlane16_swap_b32 v249, v251\n\t" \
    "s_nop 0\n\t" \
    "v_max_f32 v248, v248, v250\n\t" \
    "v_max_f32 v249, v249, v251\n\t" \
    "v_mov_b32 v250, v248\n\t" \
    "v_mov_b32 v251, v249\n\t" \
    "s_nop 1\n\t" \
    "v_permlane32_swap_b32 v248, v250\n\t" \
    "v_permlane32_swap_b32 v249, v251\n\t" \
    "s_nop 0\n\t" \
    "v_max_f32 v248, v248, v250\n\t" \
    "v_max_f32 v249, v249, v251\n\t" \
    "v_add_f32 v248, 0x41100000, v248\n\t" \
    "v_add_f32 v249, 0x41100000, v249\n\t" \
    "v_mov_b32 v236, v248\n\t" \
    "v_mov_b32 v237, v249\n\t" \
    "v_sub_f32 v104, v104, v248\n\t" \
    "v_sub_f32 v105, v105, v248\n\t" \
    "v_sub_f32 v106, v106, v248\n\t" \
    "v_sub_f32 v107, v107, v248\n\t" \
    "v_sub_f32 v108, v108, v248\n\t" \
    "v_sub_f32 v109, v109, v248\n\t" \
    "v_sub_f32 v110, v110, v248\n\t" \
    "v_sub_f32 v111, v111, v248\n\t" \
    "v_sub_f32 v112, v112, v248\n\t" \
    "v_sub_f32 v113, v113, v248\n\t" \
    "v_sub_f32 v114, v114, v248\n\t" \
    "v_sub_f32 v115, v115, v248\n\t" \
    "v_sub_f32 v116, v116, v248\n\t" \
    "v_sub_f32 v117, v117, v248\n\t" \
    "v_sub_f32 v118, v118, v248\n\t" \
    "v_sub_f32 v119, v119, v248\n\t" \
    "v_sub_f32 v136, 0, v236\n\t" \
    "v_sub_f32 v137, 0, v236\n\t" \
    "v_sub_f32 v138, 0, v236\n\t" \
    "v_sub_f32 v139, 0, v236\n\t" \
    "v_sub_f32 v120, v120, v249\n\t" \
    "v_sub_f32 v121, v121, v249\n\t" \
    "v_sub_f32 v122, v122, v249\n\t" \
    "v_sub_f32 v123, v123, v249\n\t" \
    "v_sub_f32 v124, v124, v249\n\t" \
    "v_sub_f32 v125, v125, v249\n\t" \
    "v_sub_f32 v126, v126, v249\n\t" \
    "v_sub_f32 v127, v127, v249\n\t" \
    "v_sub_f32 v128, v128, v249\n\t" \
    "v_sub_f32 v129, v129, v249\n\t" \
    "v_sub_f32 v130, v130, v249\n\t" \
    "v_sub_f32 v131, v131, v249\n\t" \
    "v_sub_f32 v132, v132, v249\n\t" \
    "v_sub_f32 v133, v133, v249\n\t" \
    "v_sub_f32 v134, v134, v249\n\t" \
    "v_sub_f32 v135, v135, v249\n\t" \
    "v_sub_f32 v140, 0, v237\n\t" \
    "v_sub_f32 v141, 0, v237\n\t" \
    "v_sub_f32 v142, 0, v237\n\t" \
    "v_sub_f32 v143, 0, v237\n\t" \
    "s_branch L_exp%=\n\t" \
    "L_post%=:\n\t" \
    "s_cmp_eq_u32 s47, 16\n\t" \
    "s_cbranch_scc1 L_last%=\n\t" \
    "s_add_u32 s52, s47, 1\n\t" \
    "s_bitcmp1_b32 s48, s52\n\t" \
    "s_cbranch_scc1 L_last%=\n\t" \
    "s_cmp_eq_u32 s45, 0\n\t" \
    "s_cbranch_scc1 L_fin%=\n\t" \
    "s_barrier\n\t" \
    "s_branch L_vend%=\n\t" \
    "L_last%=:\n\t" \
    "ds_read_b128 v[168:171], v233 offset:32768\n\t" \
    "ds_read_b128 v[172:175], v233 offset:33792\n\t" \
    "ds_read_b128 v[176:179], v233 offset:34816\n\t" \
    "ds_read_b128 v[180:183], v233 offset:35840\n\t" \
    "ds_read_b128 v[184:187], v233 offset:36864\n\t" \
    "ds_read_b128 v[188:191], v233 offset:37888\n\t" \
    "s_add_u32 s47, s47, 1\n\t" \
    "s_barrier\n\t" \
    "s_setprio 1\n\t" \
    "s_bitcmp1_b32 s83, s47\n\t" \
    "s_cbranch_scc1 L_pvz%=\n\t" \
    "s_branch L_pv%=\n\t" \
    "L_rare%=:\n\t" \
    "v_max3_f32 v248, v104, v105, v106\n\t" \
    "v_max3_f32 v249, v120, v121, v122\n\t" \
    "v_max3_f32 v248, v248, v107, v108\n\t" \
    "v_max3_f32 v249, v249, v123, v124\n\t" \
    "v_max3_f32 v248, v248, v109, v110\n\t" \
    "v_max3_f32 v249, v249, v125, v126\n\t" \
    "v_max3_f32 v248, v248, v111, v112\n\t" \
    "v_max3_f32 v249, v249, v127, v128\n\t" \
    "v_max3_f32 v248, v248, v113, v114\n\t" \
    "v_max3_f32 v249, v249, v129, v130\n\t" \
    "v_max3_f32 v248, v248, v115, v116\n\t" \
    "v_max3_f32 v249, v249, v131, v132\n\t" \
    "v_max3_f32 v248, v248, v117, v118\n\t" \
    "v_max3_f32 v249, v249, v133, v134\n\t" \
    "v_max_f32 v248, v248, v119\n\t" \
    "v_max_f32 v249, v249, v135\n\t" \
    "v_mov_b32 v250, v248\n\t" \
    "v_mov_b32 v251, v249\n\t" \
    "s_nop 1\n\t" \
    "v_permlane16_swap_b32 v248, v250\n\t" \
    "v_permlane16_swap_b32 v249, v251\n\t" \
    "s_nop 0\n\t" \
    "v_max_f32 v248, v248, v250\n\t" \
    "v_max_f32 v249, v249, v251\n\t" \
    "v_mov_b32 v250, v248\n\t" \
    "v_mov_b32 v251, v249\n\t" \
    "s_nop 1\n\t" \
    "v_permlane32_swap_b32 v248, v250\n\t" \
    "v_permlane32_swap_b32 v249, v251\n\t" \
    "s_nop 0\n\t" \
    "v_max_f32 v248, v248, v250\n\t" \
    "v_max_f32 v249, v249, v251\n\t" \
    "v_add_f32 v248, 0x41100000, v248\n\t" \
    "v_add_f32 v249, 0x41100000, v249\n\t" \
    "v_max_f32 v248, 0, v248\n\t" \
    "v_add_f32 v236, v236, v248\n\t" \
    "v_exp_f32 v250, -v248\n\t" \
    "v_max_f32 v249, 0, v249\n\t" \
    "v_add_f32 v237, v237, v249\n\t" \
    "v_exp_f32 v251, -v249\n\t" \
    "s_nop 0\n\t" \
    "v_mul_f32 v240, v240, v250\n\t" \
    "v_mul_f32 v40, v40, v250\n\t" \
    "v_mul_f32 v41, v41, v250\n\t" \
    "v_mul_f32 v42, v42, v250\n\t" \
    "v_mul_f32 v43, v43, v250\n\t" \
    "v_mul_f32 v44, v44, v250\n\t" \
    "v_mul_f32 v45, v45, v250\n\t" \
    "v_mul_f32 v46, v46, v250\n\t" \
    "v_mul_f32 v47, v47, v250\n\t" \
    "v_mul_f32 v48, v48, v250\n\t" \
    "v_mul_f32 v49, v49, v250\n\t" \
    "v_mul_f32 v50, v50, v250\n\t" \
    "v_mul_f32 v51, v51, v250\n\t" \
    "v_mul_f32 v52, v52, v250\n\t" \
    "v_mul_f32 v53, v53, v250\n\t" \
    "v_mul_f32 v54, v54, v250\n\t" \
    "v_mul_f32 v55, v55, v250\n\t" \
    "v_mul_f32 v56, v56, v250\n\t" \
    "v_mul_f32 v57, v57, v250\n\t" \
    "v_mul_f32 v58, v58, v250\n\t" \
    "v_mul_f32 v59, v59, v250\n\t" \
    "v_mul_f32 v60, v60, v250\n\t" \
    "v_mul_f32 v61, v61, v250\n\t" \
    "v_mul_f32 v62, v62, v250\n\t" \
    "v_mul_f32 v63, v63, v250\n\t" \
    "v_mul_f32 v64, v64, v250\n\t" \
    "v_mul_f32 v65, v65, v250\n\t" \
    "v_mul_f32 v66, v66, v250\n\t" \
    "v_mul_f32 v67, v67, v250\n\t" \
    "v_mul_f32 v68, v68, v250\n\t" \
    "v_mul_f32 v69, v69, v250\n\t" \
    "v_mul_f32 v70, v70, v250\n\t" \
    "v_mul_f32 v71, v71, v250\n\t" \
    "v_mul_f32 v244, v244, v251\n\t" \
    "v_mul_f32 v72, v72, v251\n\t" \
    "v_mul_f32 v73, v73, v251\n\t" \
    "v_mul_f32 v74, v74, v251\n\t" \
    "v_mul_f32 v75, v75, v251\n\t" \
    "v_mul_f32 v76, v76, v251\n\t" \
    "v_mul_f32 v77, v77, v251\n\t" \
    "v_mul_f32 v78, v78, v251\n\t" \
    "v_mul_f32 v79, v79, v251\n\t" \
    "v_mul_f32 v80, v80, v251\n\t" \
    "v_mul_f32 v81, v81, v251\n\t" \
    "v_mul_f32 v82, v82, v251\n\t" \
    "v_mul_f32 v83, v83, v251\n\t" \
    "v_mul_f32 v84, v84, v251\n\t" \
    "v_mul_f32 v85, v85, v251\n\t" \
    "v_mul_f32 v86, v86, v251\n\t" \
    "v_mul_f32 v87, v87, v251\n\t" \
    "v_mul_f32 v88, v88, v251\n\t" \
    "v_mul_f32 v89, v89, v251\n\t" \
    "v_mul_f32 v90, v90, v251\n\t" \
    "v_mul_f32 v91, v91, v251\n\t" \
    "v_mul_f32 v92, v92, v251\n\t" \
    "v_mul_f32 v93, v93, v251\n\t" \
    "v_mul_f32 v94, v94, v251\n\t" \
    "v_mul_f32 v95, v95, v251\n\t" \
    "v_mul_f32 v96, v96, v251\n\t" \
    "v_mul_f32 v97, v97, v251\n\t" \
    "v_mul_f32 v98, v98, v251\n\t" \
    "v_mul_f32 v99, v99, v251\n\t" \
    "v_mul_f32 v100, v100, v251\n\t" \
    "v_mul_f32 v101, v101, v251\n\t" \
    "v_mul_f32 v102, v102, v251\n\t" \
    "v_mul_f32 v103, v103, v251\n\t" \
    "v_sub_f32 v104, v104, v248\n\t" \
    "v_sub_f32 v105, v105, v248\n\t" \
    "v_sub_f32 v106, v106, v248\n\t" \
    "v_sub_f32 v107, v107, v248\n\t" \
    "v_sub_f32 v108, v108, v248\n\t" \
    "v_sub_f32 v109, v109, v248\n\t" \
    "v_sub_f32 v110, v110, v248\n\t" \
    "v_sub_f32 v111, v111, v248\n\t" \
    "v_sub_f32 v112, v112, v248\n\t" \
    "v_sub_f32 v113, v113, v248\n\t" \
    "v_sub_f32 v114, v114, v248\n\t" \
    "v_sub_f32 v115, v115, v248\n\t" \
    "v_sub_f32 v116, v116, v248\n\t" \
    "v_sub_f32 v117, v117, v248\n\t" \
    "v_sub_f32 v118, v118, v248\n\t" \
    "v_sub_f32 v119, v119, v248\n\t" \
    "v_sub_f32 v136, 0, v236\n\t" \
    "v_sub_f32 v137, 0, v236\n\t" \
    "v_sub_f32 v138, 0, v236\n\t" \
    "v_sub_f32 v139, 0, v236\n\t" \
    "v_sub_f32 v120, v120, v249\n\t" \
    "v_sub_f32 v121, v121, v249\n\t" \
    "v_sub_f32 v122, v122, v249\n\t" \
    "v_sub_f32 v123, v123, v249\n\t" \
    "v_sub_f32 v124, v124, v249\n\t" \
    "v_sub_f32 v125, v125, v249\n\t" \
    "v_sub_f32 v126, v126, v249\n\t" \
    "v_sub_f32 v127, v127, v249\n\t" \
    "v_sub_f32 v128, v128, v249\n\t" \
    "v_sub_f32 v129, v129, v249\n\t" \
    "v_sub_f32 v130, v130, v249\n\t" \
    "v_sub_f32 v131, v131, v249\n\t" \
    "v_sub_f32 v132, v132, v249\n\t" \
    "v_sub_f32 v133, v133, v249\n\t" \
    "v_sub_f32 v134, v134, v249\n\t" \
    "v_sub_f32 v135, v135, v249\n\t" \
    "v_sub_f32 v140, 0, v237\n\t" \
    "v_sub_f32 v141, 0, v237\n\t" \
    "v_sub_f32 v142, 0, v237\n\t" \
    "v_sub_f32 v143, 0, v237\n\t" \
    "v_exp_f32 v200, v104\n\t" \
    "v_exp_f32 v216, v120\n\t" \
    "v_exp_f32 v201, v105\n\t" \
    "v_exp_f32 v217, v121\n\t" \
    "v_exp_f32 v202, v106\n\t" \
    "v_exp_f32 v218, v122\n\t" \
    "v_exp_f32 v203, v107\n\t" \
    "v_cvt_pk_bf16_f32 v152, v200, v201\n\t" \
    "v_cvt_pk_bf16_f32 v160, v216, v217\n\t" \
    "v_exp_f32 v219, v123\n\t" \
    "v_exp_f32 v204, v108\n\t" \
    "v_exp_f32 v220, v124\n\t" \
    "v_exp_f32 v205, v109\n\t" \
    "v_cvt_pk_bf16_f32 v153, v202, v203\n\t" \
    "v_cvt_pk_bf16_f32 v161, v218, v219\n\t" \
    "v_exp_f32 v221, v125\n\t" \
    "v_exp_f32 v206, v110\n\t" \
    "v_exp_f32 v222, v126\n\t" \
    "v_exp_f32 v207, v111\n\t" \
    "v_cvt_pk_bf16_f32 v154, v204, v205\n\t" \
    "v_cvt_pk_bf16_f32 v162, v220, v221\n\t" \
    "v_exp_f32 v223, v127\n\t" \
    "v_exp_f32 v208, v112\n\t" \
    "v_or_b32 v248, v152, v160\n\t" \
    "v_or3_b32 v248, v248, v153, v161\n\t" \
    "v_exp_f32 v224, v128\n\t" \
    "v_exp_f32 v209, v113\n\t" \
    "v_cvt_pk_bf16_f32 v155, v206, v207\n\t" \
    "v_cvt_pk_bf16_f32 v163, v222, v223\n\t" \
    "v_exp_f32 v225, v129\n\t" \
    "v_exp_f32 v210, v114\n\t" \
    "v_exp_f32 v226, v130\n\t" \
    "v_exp_f32 v211, v115\n\t" \
    "v_cvt_pk_bf16_f32 v156, v208, v209\n\t" \
    "v_cvt_pk_bf16_f32 v164, v224, v225\n\t" \
    "v_exp_f32 v227, v131\n\t" \
    "v_exp_f32 v212, v116\n\t" \
    "v_or3_b32 v248, v248, v154, v162\n\t" \
    "v_or3_b32 v248, v248, v155, v163\n\t" \
    "v_exp_f32 v228, v132\n\t" \
    "v_exp_f32 v213, v117\n\t" \
    "v_cvt_pk_bf16_f32 v157, v210, v211\n\t" \
    "v_cvt_pk_bf16_f32 v165, v226, v227\n\t" \
    "v_exp_f32 v229, v133\n\t" \
    "v_exp_f32 v214, v118\n\t" \
    "v_exp_f32 v230, v134\n\t" \
    "v_exp_f32 v215, v119\n\t" \
    "v_cvt_pk_bf16_f32 v158, v212, v213\n\t" \
    "v_cvt_pk_bf16_f32 v166, v228, v229\n\t" \
    "v_exp_f32 v231, v135\n\t" \
    "v_cvt_pk_bf16_f32 v159, v214, v215\n\t" \
    "v_or3_b32 v248, v248, v156, v164\n\t" \
    "v_cvt_pk_bf16_f32 v167, v230, v231\n\t" \
    "v_or3_b32 v248, v248, v157, v165\n\t" \
    "v_or3_b32 v248, v248, v158, v166\n\t" \
    "v_or3_b32 v248, v248, v159, v167\n\t" \
    "s_branch L_sumdone%=\n\t" \
    "L_fin%=:\n\t" \
    "ds_read_b128 v[8:11], v235 offset:0\n\t" \
    "ds_read_b128 v[12:15], v235 offset:1024\n\t" \
    "ds_read_b128 v[16:19], v235 offset:2048\n\t" \
    "ds_read_b128 v[20:23], v235 offset:3072\n\t" \
    "ds_read_b128 v[24:27], v235 offset:4096\n\t" \
    "ds_read_b128 v[28:31], v235 offset:5120\n\t" \
    "ds_read_b128 v[32:35], v235 offset:6144\n\t" \
    "ds_read_b128 v[36:39], v235 offset:7168\n\t" \
    "v_mov_b32 v136, 0\n\t" \
    "v_mov_b32 v137, 0\n\t" \
    "v_mov_b32 v138, 0\n\t" \
    "v_mov_b32 v139, 0\n\t" \
    "v_mov_b32 v140, 0\n\t" \
    "v_mov_b32 v141, 0\n\t" \
    "v_mov_b32 v142, 0\n\t" \
    "v_mov_b32 v143, 0\n\t" \
    "s_waitcnt lgkmcnt(0)\n\t" \
    "s_branch L_vend%=\n\t" \
    "L_final%=:\n\t" \
    "s_lshr_b32 s52, s46, 1\n\t" \
    "s_lshl_b32 s52, s52, 16\n\t" \
    "s_and_b32 s53, s46, 1\n\t" \
    "s_lshl_b32 s53, s53, 14\n\t" \
    "s_add_u32 s52, s52, s53\n\t" \
    "s_add_u32 s52, s52, s49\n\t" \
    "v_add_u32 v254, s52, v234\n\t" \
    "s_nop 4\n\t" \
    "s_cmp_lg_u32 s45, 0\n\t" \
    "s_cbranch_scc1 L_final1%=\n\t" \
    "v_lshrrev_b32 v252, 1, v234\n\t" \
    "v_and_b32 v252, 0xf8, v252\n\t" \
    "global_load_dwordx2 v[250:251], v252, %12 sc1\n\t" \
    "ds_write_b128 v254, v[72:75] offset:0\n\t" \
    "ds_write_b128 v254, v[76:79] offset:1024\n\t" \
    "ds_write_b128 v254, v[80:83] offset:2048\n\t" \
    "ds_write_b128 v254, v[84:87] offset:3072\n\t" \
    "ds_write_b128 v254, v[88:91] offset:4096\n\t" \
    "ds_write_b128 v254, v[92:95] offset:5120\n\t" \
    "ds_write_b128 v254, v[96:99] offset:6144\n\t" \
    "ds_write_b128 v254, v[100:103] offset:7168\n\t" \
    "v_mov_b32 v144, v237\n\t" \
    "v_mov_b32 v145, v244\n\t" \
    "ds_write_b64 v235, v[144:145]\n\t" \
    "s_waitcnt lgkmcnt(0)\n\t" \
    "s_barrier\n\t" \
    "s_mov_b32 s54, 0x100000\n\t" \
    "L_f1%=:\n\t" \
    "s_waitcnt vmcnt(0)\n\t" \
    "v_xor_b32 v250, %10, v250\n\t" \
    "v_xor_b32 v251, %11, v251\n\t" \
    "v_or_b32 v250, v250, v251\n\t" \
    "v_cmp_ne_u32 vcc, 0, v250\n\t" \
    "s_cbranch_vccz L_f1ok%=\n\t" \
    "s_sub_u32 s54, s54, 1\n\t" \
    "s_cbranch_scc1 L_f1ok%=\n\t" \
    "s_sleep 2\n\t" \
    "global_load_dwordx2 v[250:251], v252, %12 sc1\n\t" \
    "s_branch L_f1%=\n\t" \
    "L_f1ok%=:\n\t" \
    "v_add_u32 v255, 180224, v234\n\t" \
    "s_mov_b64 s[56:57], %13\n\t" \
    "s_mov_b64 s[58:59], %14\n\t" \
    "s_lshl_b32 s52, s46, 11\n\t" \
    "s_add_u32 s52, s52, s49\n\t" \
    "s_add_u32 m0, s52, 32768\n\t" \
    "s_nop 0\n\t" \
    "global_load_lds_dwordx4 v234, s[56:57] sc1\n\t" \
    "s_add_u32 m0, s52, 33792\n\t" \
    "s_nop 0\n\t" \
    "global_load_lds_dwordx4 v255, s[56:57] sc1\n\t" \
    "s_add_u32 m0, s52, 40960\n\t" \
    "s_nop 0\n\t" \
    "global_load_lds_dwordx4 %15, s[58:59]\n\t" \
    "s_add_u32 m0, s52, 41984\n\t" \
    "s_nop 0\n\t" \
    "global_load_lds_dwordx4 %16, s[58:59]\n\t" \
    "s_add_u32 s56, s56, 1024\n\t" \
    "s_addc_u32 s57, s57, 0\n\t" \
    "s_add_u32 s58, s58, 32768\n\t" \
    "s_addc_u32 s59, s59, 0\n\t" \
    "s_add_u32 m0, s52, 49152\n\t" \
    "s_nop 0\n\t" \
    "global_load_lds_dwordx4 v234, s[56:57] sc1\n\t" \
    "s_add_u32 m0, s52, 50176\n\t" \
    "s_nop 0\n\t" \
    "global_load_lds_dwordx4 v255, s[56:57] sc1\n\t" \
    "s_add_u32 m0, s52, 57344\n\t" \
    "s_nop 0\n\t" \
    "global_load_lds_dwordx4 %15, s[58:59]\n\t" \
    "s_add_u32 m0, s52, 58368\n\t" \
    "s_nop 0\n\t" \
    "global_load_lds_dwordx4 %16, s[58:59]\n\t" \
    "s_add_u32 s56, s56, 1024\n\t" \
    "s_addc_u32 s57, s57, 0\n\t" \
    "s_add_u32 s58, s58, 32768\n\t" \
    "s_addc_u32 s59, s59, 0\n\t" \
    "s_add_u32 m0, s52, 98304\n\t" \
    "s_nop 0\n\t" \
    "global_load_lds_dwordx4 v234, s[56:57] sc1\n\t" \
    "s_add_u32 m0, s52, 99328\n\t" \
    "s_nop 0\n\t" \
    "global_load_lds_dwordx4 v255, s[56:57] sc1\n\t" \
    "s_add_u32 m0, s52, 106496\n\t" \
    "s_nop 0\n\t" \
    "global_load_lds_dwordx4 %15, s[58:59]\n\t" \
    "s_add_u32 m0, s52, 107520\n\t" \
    "s_nop 0\n\t" \
    "global_load_lds_dwordx4 %16, s[58:59]\n\t" \
    "s_add_u32 s56, s56, 1024\n\t" \
    "s_addc_u32 s57, s57, 0\n\t" \
    "s_add_u32 s58, s58, 32768\n\t" \
    "s_addc_u32 s59, s59, 0\n\t" \
    "s_add_u32 m0, s52, 114688\n\t" \
    "s_nop 0\n\t" \
    "global_load_lds_dwordx4 v234, s[56:57] sc1\n\t" \
    "s_add_u32 m0, s52, 115712\n\t" \
    "s_nop 0\n\t" \
    "global_load_lds_dwordx4 v255, s[56:57] sc1\n\t" \
    "s_add_u32 m0, s52, 122880\n\t" \
    "s_nop 0\n\t" \
    "global_load_lds_dwordx4 %15, s[58:59]\n\t" \
    "s_add_u32 m0, s52, 123904\n\t" \
    "s_nop 0\n\t" \
    "global_load_lds_dwordx4 %16, s[58:59]\n\t" \
    "s_barrier\n\t" \
    "ds_read_b64 v[144:145], v235 offset:1024\n\t" \
    "ds_read_b128 v[200:203], v254 offset:8192\n\t" \
    "ds_read_b128 v[204:207], v254 offset:9216\n\t" \
    "ds_read_b128 v[208:211], v254 offset:10240\n\t" \
    "ds_read_b128 v[212:215], v254 offset:11264\n\t" \
    "ds_read_b128 v[216:219], v254 offset:12288\n\t" \
    "ds_read_b128 v[220:223], v254 offset:13312\n\t" \
    "ds_read_b128 v[224:227], v254 offset:14336\n\t" \
    "ds_read_b128 v[228:231], v254 offset:15360\n\t" \
    "s_waitcnt lgkmcnt(8)\n\t" \
    "v_max_f32 v250, v144, v236\n\t" \
    "v_sub_f32 v248, v144, v250\n\t" \
    "v_sub_f32 v249, v236, v250\n\t" \
    "v_exp_f32 v248, v248\n\t" \
    "v_exp_f32 v249, v249\n\t" \
    "s_nop 0\n\t" \
    "v_mul_f32 v250, v145, v248\n\t" \
    "v_fmac_f32 v250, v240, v249\n\t" \
    "v_rcp_f32 v250, v250\n\t" \
    "s_nop 0\n\t" \
    "v_mul_f32 v248, v248, v250\n\t" \
    "v_mul_f32 v249, v249, v250\n\t" \
    "v_mul_f32 v40, v40, v249\n\t" \
    "v_mul_f32 v41, v41, v249\n\t" \
    "v_mul_f32 v42, v42, v249\n\t" \
    "v_mul_f32 v43, v43, v249\n\t" \
    "v_mul_f32 v44, v44, v249\n\t" \
    "v_mul_f32 v45, v45, v249\n\t" \
    "v_mul_f32 v46, v46, v249\n\t" \
    "v_mul_f32 v47, v47, v249\n\t" \
    "v_mul_f32 v48, v48, v249\n\t" \
    "v_mul_f32 v49, v49, v249\n\t" \
    "v_mul_f32 v50, v50, v249\n\t" \
    "v_mul_f32 v51, v51, v249\n\t" \
    "v_mul_f32 v52, v52, v249\n\t" \
    "v_mul_f32 v53, v53, v249\n\t" \
    "v_mul_f32 v54, v54, v249\n\t" \
    "v_mul_f32 v55, v55, v249\n\t" \
    "v_mul_f32 v56, v56, v249\n\t" \
    "v_mul_f32 v57, v57, v249\n\t" \
    "v_mul_f32 v58, v58, v249\n\t" \
    "v_mul_f32 v59, v59, v249\n\t" \
    "v_mul_f32 v60, v60, v249\n\t" \
    "v_mul_f32 v61, v61, v249\n\t" \
    "v_mul_f32 v62, v62, v249\n\t" \
    "v_mul_f32 v63, v63, v249\n\t" \
    "v_mul_f32 v64, v64, v249\n\t" \
    "v_mul_f32 v65, v65, v249\n\t" \
    "v_mul_f32 v66, v66, v249\n\t" \
    "v_mul_f32 v67, v67, v249\n\t" \
    "v_mul_f32 v68, v68, v249\n\t" \
    "v_mul_f32 v69, v69, v249\n\t" \
    "v_mul_f32 v70, v70, v249\n\t" \
    "v_mul_f32 v71, v71, v249\n\t" \
    "s_waitcnt lgkmcnt(0)\n\t" \
    "v_fmac_f32 v40, v200, v248\n\t" \
    "v_fmac_f32 v41, v201, v248\n\t" \
    "v_fmac_f32 v42, v202, v248\n\t" \
    "v_fmac_f32 v43, v203, v248\n\t" \
    "v_fmac_f32 v44, v204, v248\n\t" \
    "v_fmac_f32 v45, v205, v248\n\t" \
    "v_fmac_f32 v46, v206, v248\n\t" \
    "v_fmac_f32 v47, v207, v248\n\t" \
    "v_cvt_pk_bf16_f32 v104, v40, v41\n\t" \
    "v_cvt_pk_bf16_f32 v105, v42, v43\n\t" \
    "v_cvt_pk_bf16_f32 v106, v44, v45\n\t" \
    "v_cvt_pk_bf16_f32 v107, v46, v47\n\t" \
    "global_store_dwordx4 v234, v[104:107], s[42:43] offset:0 sc1\n\t" \
    "v_fmac_f32 v48, v208, v248\n\t" \
    "v_fmac_f32 v49, v209, v248\n\t" \
    "v_fmac_f32 v50, v210, v248\n\t" \
    "v_fmac_f32 v51, v211, v248\n\t" \
    "v_fmac_f32 v52, v212, v248\n\t" \
    "v_fmac_f32 v53, v213, v248\n\t" \
    "v_fmac_f32 v54, v214, v248\n\t" \
    "v_fmac_f32 v55, v215, v248\n\t" \
    "v_cvt_pk_bf16_f32 v108, v48, v49\n\t" \
    "v_cvt_pk_bf16_f32 v109, v50, v51\n\t" \
    "v_cvt_pk_bf16_f32 v110, v52, v53\n\t" \
    "v_cvt_pk_bf16_f32 v111, v54, v55\n\t" \
    "global_store_dwordx4 v234, v[108:111], s[42:43] offset:1024 sc1\n\t" \
    "v_fmac_f32 v56, v216, v248\n\t" \
    "v_fmac_f32 v57, v217, v248\n\t" \
    "v_fmac_f32 v58, v218, v248\n\t" \
    "v_fmac_f32 v59, v219, v248\n\t" \
    "v_fmac_f32 v60, v220, v248\n\t" \
    "v_fmac_f32 v61, v221, v248\n\t" \
    "v_fmac_f32 v62, v222, v248\n\t" \
    "v_fmac_f32 v63, v223, v248\n\t" \
    "v_cvt_pk_bf16_f32 v112, v56, v57\n\t" \
    "v_cvt_pk_bf16_f32 v113, v58, v59\n\t" \
    "v_cvt_pk_bf16_f32 v114, v60, v61\n\t" \
    "v_cvt_pk_bf16_f32 v115, v62, v63\n\t" \
    "global_store_dwordx4 v234, v[112:115], s[42:43] offset:2048 sc1\n\t" \
    "v_fmac_f32 v64, v224, v248\n\t" \
    "v_fmac_f32 v65, v225, v248\n\t" \
    "v_fmac_f32 v66, v226, v248\n\t" \
    "v_fmac_f32 v67, v227, v248\n\t" \
    "v_fmac_f32 v68, v228, v248\n\t" \
    "v_fmac_f32 v69, v229, v248\n\t" \
    "v_fmac_f32 v70, v230, v248\n\t" \
    "v_fmac_f32 v71, v231, v248\n\t" \
    "v_cvt_pk_bf16_f32 v116, v64, v65\n\t" \
    "v_cvt_pk_bf16_f32 v117, v66, v67\n\t" \
    "v_cvt_pk_bf16_f32 v118, v68, v69\n\t" \
    "v_cvt_pk_bf16_f32 v119, v70, v71\n\t" \
    "global_store_dwordx4 v234, v[116:119], s[42:43] offset:3072 sc1\n\t" \
    "s_nop 1\n\t" \
    "s_branch L_end%=\n\t" \
    "L_final1%=:\n\t" \
    "ds_write_b128 v254, v[40:43] offset:8192\n\t" \
    "ds_write_b128 v254, v[44:47] offset:9216\n\t" \
    "ds_write_b128 v254, v[48:51] offset:10240\n\t" \
    "ds_write_b128 v254, v[52:55] offset:11264\n\t" \
    "ds_write_b128 v254, v[56:59] offset:12288\n\t" \
    "ds_write_b128 v254, v[60:63] offset:13312\n\t" \
    "ds_write_b128 v254, v[64:67] offset:14336\n\t" \
    "ds_write_b128 v254, v[68:71] offset:15360\n\t" \
    "v_mov_b32 v146, v236\n\t" \
    "v_mov_b32 v147, v240\n\t" \
    "ds_write_b64 v235, v[146:147] offset:1024\n\t" \
    "ds_read_b64 v[144:145], v235\n\t" \
    "ds_read_b128 v[200:203], v254 offset:0\n\t" \
    "ds_read_b128 v[204:207], v254 offset:1024\n\t" \
    "ds_read_b128 v[208:211], v254 offset:2048\n\t" \
    "ds_read_b128 v[212:215], v254 offset:3072\n\t" \
    "ds_read_b128 v[216:219], v254 offset:4096\n\t" \
    "ds_read_b128 v[220:223], v254 offset:5120\n\t" \
    "ds_read_b128 v[224:227], v254 offset:6144\n\t" \
    "ds_read_b128 v[228:231], v254 offset:7168\n\t" \
    "s_waitcnt lgkmcnt(8)\n\t" \
    "s_barrier\n\t" \
    "v_max_f32 v250, v144, v237\n\t" \
    "v_sub_f32 v248, v144, v250\n\t" \
    "v_sub_f32 v249, v237, v250\n\t" \
    "v_exp_f32 v248, v248\n\t" \
    "v_exp_f32 v249, v249\n\t" \
    "s_nop 0\n\t" \
    "v_mul_f32 v250, v145, v248\n\t" \
    "v_fmac_f32 v250, v244, v249\n\t" \
    "v_rcp_f32 v250, v250\n\t" \
    "s_nop 0\n\t" \
    "v_mul_f32 v248, v248, v250\n\t" \
    "v_mul_f32 v249, v249, v250\n\t" \
    "v_mul_f32 v72, v72, v249\n\t" \
    "v_mul_f32 v73, v73, v249\n\t" \
    "v_mul_f32 v74, v74, v249\n\t" \
    "v_mul_f32 v75, v75, v249\n\t" \
    "v_mul_f32 v76, v76, v249\n\t" \
    "v_mul_f32 v77, v77, v249\n\t" \
    "v_mul_f32 v78, v78, v249\n\t" \
    "v_mul_f32 v79, v79, v249\n\t" \
    "v_mul_f32 v80, v80, v249\n\t" \
    "v_mul_f32 v81, v81, v249\n\t" \
    "v_mul_f32 v82, v82, v249\n\t" \
    "v_mul_f32 v83, v83, v249\n\t" \
    "v_mul_f32 v84, v84, v249\n\t" \
    "v_mul_f32 v85, v85, v249\n\t" \
    "v_mul_f32 v86, v86, v249\n\t" \
    "v_mul_f32 v87, v87, v249\n\t" \
    "v_mul_f32 v88, v88, v249\n\t" \
    "v_mul_f32 v89, v89, v249\n\t" \
    "v_mul_f32 v90, v90, v249\n\t" \
    "v_mul_f32 v91, v91, v249\n\t" \
    "v_mul_f32 v92, v92, v249\n\t" \
    "v_mul_f32 v93, v93, v249\n\t" \
    "v_mul_f32 v94, v94, v249\n\t" \
    "v_mul_f32 v95, v95, v249\n\t" \
    "v_mul_f32 v96, v96, v249\n\t" \
    "v_mul_f32 v97, v97, v249\n\t" \
    "v_mul_f32 v98, v98, v249\n\t" \
    "v_mul_f32 v99, v99, v249\n\t" \
    "v_mul_f32 v100, v100, v249\n\t" \
    "v_mul_f32 v101, v101, v249\n\t" \
    "v_mul_f32 v102, v102, v249\n\t" \
    "v_mul_f32 v103, v103, v249\n\t" \
    "s_waitcnt lgkmcnt(0)\n\t" \
    "v_add_u32 v255, 180224, v234\n\t" \
    "v_fmac_f32 v72, v200, v248\n\t" \
    "v_fmac_f32 v73, v201, v248\n\t" \
    "v_fmac_f32 v74, v202, v248\n\t" \
    "v_fmac_f32 v75, v203, v248\n\t" \
    "v_fmac_f32 v76, v204, v248\n\t" \
    "v_fmac_f32 v77, v205, v248\n\t" \
    "v_fmac_f32 v78, v206, v248\n\t" \
    "v_fmac_f32 v79, v207, v248\n\t" \
    "v_cvt_pk_bf16_f32 v104, v72, v73\n\t" \
    "v_cvt_pk_bf16_f32 v105, v74, v75\n\t" \
    "v_cvt_pk_bf16_f32 v106, v76, v77\n\t" \
    "v_cvt_pk_bf16_f32 v107, v78, v79\n\t" \
    "global_store_dwordx4 v255, v[104:107], s[42:43] offset:0 sc1\n\t" \
    "v_fmac_f32 v80, v208, v248\n\t" \
    "v_fmac_f32 v81, v209, v248\n\t" \
    "v_fmac_f32 v82, v210, v248\n\t" \
    "v_fmac_f32 v83, v211, v248\n\t" \
    "v_fmac_f32 v84, v212, v248\n\t" \
    "v_fmac_f32 v85, v213, v248\n\t" \
    "v_fmac_f32 v86, v214, v248\n\t" \
    "v_fmac_f32 v87, v215, v248\n\t" \
    "v_cvt_pk_bf16_f32 v108, v80, v81\n\t" \
    "v_cvt_pk_bf16_f32 v109, v82, v83\n\t" \
    "v_cvt_pk_bf16_f32 v110, v84, v85\n\t" \
    "v_cvt_pk_bf16_f32 v111, v86, v87\n\t" \
    "global_store_dwordx4 v255, v[108:111], s[42:43] offset:1024 sc1\n\t" \
    "v_fmac_f32 v88, v216, v248\n\t" \
    "v_fmac_f32 v89, v217, v248\n\t" \
    "v_fmac_f32 v90, v218, v248\n\t" \
    "v_fmac_f32 v91, v219, v248\n\t" \
    "v_fmac_f32 v92, v220, v248\n\t" \
    "v_fmac_f32 v93, v221, v248\n\t" \
    "v_fmac_f32 v94, v222, v248\n\t" \
    "v_fmac_f32 v95, v223, v248\n\t" \
    "v_cvt_pk_bf16_f32 v112, v88, v89\n\t" \
    "v_cvt_pk_bf16_f32 v113, v90, v91\n\t" \
    "v_cvt_pk_bf16_f32 v114, v92, v93\n\t" \
    "v_cvt_pk_bf16_f32 v115, v94, v95\n\t" \
    "global_store_dwordx4 v255, v[112:115], s[42:43] offset:2048 sc1\n\t" \
    "v_fmac_f32 v96, v224, v248\n\t" \
    "v_fmac_f32 v97, v225, v248\n\t" \
    "v_fmac_f32 v98, v226, v248\n\t" \
    "v_fmac_f32 v99, v227, v248\n\t" \
    "v_fmac_f32 v100, v228, v248\n\t" \
    "v_fmac_f32 v101, v229, v248\n\t" \
    "v_fmac_f32 v102, v230, v248\n\t" \
    "v_fmac_f32 v103, v231, v248\n\t" \
    "v_cvt_pk_bf16_f32 v116, v96, v97\n\t" \
    "v_cvt_pk_bf16_f32 v117, v98, v99\n\t" \
    "v_cvt_pk_bf16_f32 v118, v100, v101\n\t" \
    "v_cvt_pk_bf16_f32 v119, v102, v103\n\t" \
    "global_store_dwordx4 v255, v[116:119], s[42:43] offset:3072 sc1\n\t" \
    "s_nop 1\n\t" \
    "L_end%=:\n\t"
#define ATTN_CLOBBERS "v8", "v9", "v10", "v11", "v12", "v13", "v14", "v15", "v16", "v17", "v18", "v19", "v20", "v21", "v22", "v23", "v24", "v25", "v26", "v27", "v28", "v29", "v30", "v31", "v32", "v33", "v34", "v35", "v36", "v37", "v38", "v39", "v40", "v41", "v42", "v43", "v44", "v45", "v46", "v47", "v48", "v49", "v50", "v51", "v52", "v53", "v54", "v55", "v56", "v57", "v58", "v59", "v60", "v61", "v62", "v63", "v64", "v65", "v66", "v67", "v68", "v69", "v70", "v71", "v72", "v73", "v74", "v75", "v76", "v77", "v78", "v79", "v80", "v81", "v82", "v83", "v84", "v85", "v86", "v87", "v88", "v89", "v90", "v91", "v92", "v93", "v94", "v95", "v96", "v97", "v98", "v99", "v100", "v101", "v102", "v103", "v104", "v105", "v106", "v107", "v108", "v109", "v110", "v111", "v112", "v113", "v114", "v115", "v116", "v117", "v118", "v119", "v120", "v121", "v122", "v123", "v124", "v125", "v126", "v127", "v128", "v129", "v130", "v131", "v132", "v133", "v134", "v135", "v136", "v137", "v138", "v139", "v140", "v141", "v142", "v143", "v144", "v145", "v146", "v147", "v148", "v149", "v150", "v151", "v152", "v153", "v154", "v155", "v156", "v157", "v158", "v159", "v160", "v161", "v162", "v163", "v164", "v165", "v166", "v167", "v168", "v169", "v170", "v171", "v172", "v173", "v174", "v175", "v176", "v177", "v178", "v179", "v180", "v181", "v182", "v183", "v184", "v185", "v186", "v187", "v188", "v189", "v190", "v191", "v192", "v193", "v194", "v195", "v196", "v197", "v198", "v199", "v200", "v201", "v202", "v203", "v204", "v205", "v206", "v207", "v208", "v209", "v210", "v211", "v212", "v213", "v214", "v215", "v216", "v217", "v218", "v219", "v220", "v221", "v222", "v223", "v224", "v225", "v226", "v227", "v228", "v229", "v230", "v231", "v232", "v233", "v234", "v235", "v236", "v237", "v238", "v239", "v240", "v241", "v242", "v243", "v244", "v245", "v246", "v247", "v248", "v249", "v250", "v251", "v252", "v253", "v254", "v255", "s36", "s37", "s38", "s39", "s40", "s41", "s42", "s43", "s44", "s45", "s46", "s47", "s48", "s49", "s50", "s51", "s52", "s53", "s54", "s55", "s56", "s57", "s58", "s59", "s60", "s61", "s62", "s63", "s64", "s65", "s66", "s67", "s68", "s69", "s70", "s71", "s72", "s73", "s74", "s75", "s76", "s77", "s78", "s79", "s80", "s81", "s82", "s83", "s84", "s85", "s86", "s87", "s88", "s89", "s90", "s91", "s92", "s93", "vcc", "scc", "memory"

DI unsigned rfl(unsigned x) { return __builtin_amdgcn_readfirstlane(x); }
DI const void* uptr(const void* p) { const unsigned long long v = (unsigned long long)p; return (const void*)(((unsigned long long)rfl((unsigned)(v >> 32)) << 32) | rfl((unsigned)v)); }
DI unsigned lane_now() { unsigned l; asm volatile("v_mbcnt_lo_u32_b32 %0, -1, 0\n\tv_mbcnt_hi_u32_b32 %0, -1, %0" : "=v"(l)); return l; }
DI unsigned cvtpk(float lo, float hi) { f32x2 v = {lo, hi}; bf16x2 b = __builtin_convertvector(v, bf16x2); return __builtin_bit_cast(unsigned, b); }
DI float bflo(unsigned w) { return __uint_as_float(w << 16); }
DI float bfhi(unsigned w) { return __uint_as_float(w & 0xffff0000u); }
template <int CTRL> DI float dpp_add(float v) { return v + __uint_as_float(__builtin_amdgcn_update_dpp(0u, __float_as_uint(v), CTRL, 0xf, 0xf, true)); }
DI float wave_sum(float v) {
    v = dpp_add<0xB1>(v); v = dpp_add<0x4E>(v); v = dpp_add<0x141>(v); v = dpp_add<0x140>(v);
    auto a = __builtin_amdgcn_permlane16_swap(__float_as_uint(v), __float_as_uint(v), false, false); v = __uint_as_float(a[0]) + __uint_as_float(a[1]);
    auto c = __builtin_amdgcn_permlane32_swap(__float_as_uint(v), __float_as_uint(v), false, false); return __uint_as_float(c[0]) + __uint_as_float(c[1]);
}
DI void dma16(unsigned ldsdst, unsigned voff, const void* sbase) {
    asm volatile("s_mov_b32 m0, %0\n\ts_nop 0\n\tglobal_load_lds_dwordx4 %1, %2" :: "s"(ldsdst), "v"(voff), "s"(sbase) : "memory");
}
DI void dma16s(unsigned ldsdst, unsigned voff, const void* sbase) {
    asm volatile("s_mov_b32 m0, %0\n\ts_nop 0\n\tglobal_load_lds_dwordx4 %1, %2 sc1" :: "s"(ldsdst), "v"(voff), "s"(sbase) : "memory");
}
DI void touch_if(unsigned& d, int a, int b, unsigned voff, const void* sbase) {
    asm volatile("s_cmp_lg_u32 %3, %4\n\ts_cbranch_scc1 TE%=\n\tglobal_load_dword %0, %1, %2\n\tTE%=:" : "+v"(d) : "v"(voff), "s"(sbase), "s"(a), "i"(b) : "memory", "scc");
}
DI void touch_lt(unsigned& d, int a, int b, unsigned voff, const void* sbase) {
    asm volatile("s_cmp_ge_u32 %3, %4\n\ts_cbranch_scc1 TE%=\n\tglobal_load_dword %0, %1, %2\n\tTE%=:" : "+v"(d) : "v"(voff), "s"(sbase), "s"(a), "i"(b) : "memory", "scc");
}
template <class V> DI void store16_sc1(char* sbase, unsigned voff, V v) { asm volatile("global_store_dwordx4 %0, %1, %2 sc1\n\ts_nop 1" :: "v"(voff), "v"(v), "s"(sbase) : "memory"); }
#define wait_bar_n(n) asm volatile("s_waitcnt vmcnt(%0) lgkmcnt(0)\n\ts_barrier" :: "i"(n) : "memory")
template <int N> DI void wait_bar() { asm volatile("s_waitcnt vmcnt(%0) lgkmcnt(0)\n\ts_barrier" :: "i"(N) : "memory"); }
DI bf16x8 ldsr(lptr p) { return *(LAS bf16x8*)p; }
DI bf16x4 ldtr(lptr p) { return __builtin_bit_cast(bf16x4, __builtin_amdgcn_ds_read_tr16_b64_v4i16((LAS s16x4*)p)); }
DI bf16x8 ldtr2(lptr p, int off2) { bf16x4 a = ldtr(p), b = ldtr(p + off2); return __builtin_shufflevector(a, b, 0, 1, 2, 3, 4, 5, 6, 7); }
DI u32x4 pack2(const f32x4& a, const f32x4& b) { return (u32x4){cvtpk(a[0], a[1]), cvtpk(a[2], a[3]), cvtpk(b[0], b[1]), cvtpk(b[2], b[3])}; }

typedef unsigned long long u64;
#define W0_BEGIN "s_cmp_lg_u32 %[wid], 0\n\ts_cbranch_scc1 W0E%=\n\t"
#define W0_END "W0E%=:"
DI void w0_epoch(u32x2& tk, const void* ctr, int wid, unsigned vzero) {
    asm volatile(W0_BEGIN "global_load_dwordx2 %[tk], %[z], %[p] sc1\n\t" W0_END : [tk] "+v"(tk) : [p] "s"(ctr), [wid] "s"(wid), [z] "v"(vzero) : "memory", "scc");
}
DI u64 w0_tag(u32x2& tk) { asm volatile("" : "+v"(tk)); return (((u64)rfl(tk[1]) << 32) | rfl(tk[0])) + (1ull << 62); }
DI void w0_flag(const void* flag, u64 tag, int wid, unsigned vzero) { const u32x2 t = {(unsigned)tag, (unsigned)(tag >> 32)};
    asm volatile(W0_BEGIN "s_mov_b64 exec, 1\n\tglobal_store_dwordx2 %[z], %[t], %[p] sc1\n\ts_mov_b64 exec, -1\n\t" W0_END :: [t] "v"(t), [p] "s"(flag), [wid] "s"(wid), [z] "v"(vzero) : "memory", "scc");
}
#define W0_LD(k) "global_load_dword %[l" #k "], %[o], %[p] offset:512*" #k " sc1\n\tglobal_load_dword %[h" #k "], %[o], %[p] offset:512*" #k "+4 sc1\n\t"
#define W0_T(k) "v_xor_b32 %[t], %[a], %[l" #k "]\n\tv_xor_b32 %[u], %[b], %[h" #k "]\n\tv_or3_b32 %[r], %[r], %[t], %[u]\n\t"
#define W0_CHECK(LD, T) W0_BEGIN "s_mov_b32 %[n], 0x100000\n\tW0L%=:\n\tv_mov_b32 %[r], 0\n\t" T "v_cmp_ne_u32 vcc, 0, %[r]\n\ts_cbranch_vccz W0E%=\n\t" \
        "s_sub_u32 %[n], %[n], 1\n\ts_cbranch_scc1 W0E%=\n\ts_sleep 2\n\t" LD "s_waitcnt vmcnt(0)\n\ts_branch W0L%=\n\t" W0_END
struct Fl4 { unsigned l0, h0, l1, h1, l2, h2, l3, h3; };
DI void w0_load4(Fl4& f, const void* p, unsigned voff, int wid) {
    asm volatile(W0_BEGIN W0_LD(0) W0_LD(1) W0_LD(2) W0_LD(3) W0_END
                 : [l0] "+v"(f.l0), [h0] "+v"(f.h0), [l1] "+v"(f.l1), [h1] "+v"(f.h1), [l2] "+v"(f.l2), [h2] "+v"(f.h2), [l3] "+v"(f.l3), [h3] "+v"(f.h3) : [p] "s"(p), [o] "v"(voff), [wid] "s"(wid) : "memory", "scc");
}
DI void w0_check4(Fl4& f, const void* p, unsigned voff, u64 tag, int wid) { const u64 lo = tag; unsigned t, u, r, n;
    asm volatile(W0_CHECK(W0_LD(0) W0_LD(1) W0_LD(2) W0_LD(3), W0_T(0) W0_T(1) W0_T(2) W0_T(3))
                 : [l0] "+v"(f.l0), [h0] "+v"(f.h0), [l1] "+v"(f.l1), [h1] "+v"(f.h1), [l2] "+v"(f.l2), [h2] "+v"(f.h2), [l3] "+v"(f.l3), [h3] "+v"(f.h3), [t] "=&v"(t), [u] "=&v"(u), [r] "=&v"(r), [n] "=&s"(n)
                 : [p] "s"(p), [o] "v"(voff), [a] "s"((unsigned)lo), [b] "s"((unsigned)(lo >> 32)), [wid] "s"(wid) : "memory", "scc", "vcc");
}
DI void w0_load1(Fl4& f, const void* p, unsigned voff, int wid) {
    asm volatile(W0_BEGIN W0_LD(0) W0_END : [l0] "+v"(f.l0), [h0] "+v"(f.h0) : [p] "s"(p), [o] "v"(voff), [wid] "s"(wid) : "memory", "scc");
}
DI void w0_check1(Fl4& f, const void* p, unsigned voff, u64 tag, int wid) { const u64 lo = tag; unsigned t, u, r, n;
    asm volatile(W0_CHECK(W0_LD(0), W0_T(0)) : [l0] "+v"(f.l0), [h0] "+v"(f.h0), [t] "=&v"(t), [u] "=&v"(u), [r] "=&v"(r), [n] "=&s"(n)
                 : [p] "s"(p), [o] "v"(voff), [a] "s"((unsigned)lo), [b] "s"((unsigned)(lo >> 32)), [wid] "s"(wid) : "memory", "scc", "vcc");
}
constexpr int CODEKB = 64;

#define KSLOT(kt) ((((kt) + 3) % 5) * 32768)
#define QSLOT(kt) ((((kt) + 4) % 5) * 32768)
struct QSrc { const char* xb; const char* wqb; const char* wgb; unsigned voffq, voffg, dstg; };
#define Q_PIECE(kt, p) do { const int so_ = QSLOT(kt); \
        if ((p) == 0) dma16s(lds0 + wid * 2048 + so_, vlin, qs.xb); else if ((p) == 1) dma16s(lds0 + wid * 2048 + so_ + 1024, vlin + 16384, qs.xb); \
        else if ((p) == 2) dma16(lds0 + 16384 + wid * 1024 + so_, qs.voffq, qs.wqb); \
        else { dma16(qs.dstg + so_, qs.voffg, qs.wgb); qs.xb += 1024; qs.wqb += 32 * 8192; qs.wgb += 32 * (FF * 2); asm volatile("" : "+s"(qs.xb), "+s"(qs.wqb), "+s"(qs.wgb)); } \
        SBAR(); } while (0)

template <int PART> DI void kv_loop(lptr lds, unsigned lds0, const char* cb, const char* wb, unsigned voffc, unsigned voffw, QSrc& qs, char* ws, u32x2& tk, u64& tag,
                                    unsigned toff0, const void* tp0, unsigned& td, const void* wgp, const void* wup, char* ob, int wid, int lane, f32x4 (&acc)[8][4]) {
    const int r = lane & 15, g = lane >> 4, q = r >> 2, p4 = lane & 3;
    const int kh = (wid >> 1) & 1, dh = wid & 1;
    const unsigned vlin = lane * 16, vzero = 0;
    lptr ca = lds + (128 * kh + r) * 64 + ((g ^ ((r >> 3) * 3)) * 16);
    lptr wa[4];
#pragma unroll
    for (int dt = 0; dt < 4; ++dt) wa[dt] = lds + 16384 + PART * 8192 + (8 * g + q) * 256 + (((4 * dh + dt) ^ (q | ((g & 1) << 2))) * 32) + p4 * 8;
    const unsigned dstc = lds0 + wid * 2048, dstw = lds0 + 16384 + wid * 2048;
    bf16x8 cf[8], wf[2][4];
    Fl4 fl = {};
#define KV_RW(buf, kt, dt) do { wf[buf][dt] = ldtr2(wa[dt] + KSLOT(kt), 1024); } while (0)
#define KV_RC(kt, t) do { cf[t] = ldsr(ca + KSLOT(kt) + (t) * 1024); } while (0)
#define KV_MMA1(buf, t) do { _Pragma("unroll") for (int dt = 0; dt < 4; ++dt) \
        acc[t][dt] = PART == 0 ? MFMA(wf[buf][dt], cf[t], acc[t][dt]) : MFMA(cf[t], wf[buf][dt], acc[t][dt]); SBAR(); } while (0)
#define KV_PIECE(kt, p) do { const int so_ = KSLOT(kt); \
        if ((kt) >= 16) Q_PIECE((kt) - 16, p); \
        else { if ((p) == 0) dma16(dstc + so_, voffc, cb); else if ((p) == 1) dma16(dstc + so_ + 1024, voffc + 16384, cb); else if ((p) == 2) dma16(dstw + so_, voffw, wb); \
               else { dma16(dstw + so_ + 1024, voffw + 32768, wb); cb += 64; wb += 32 * 8192; asm volatile("" : "+s"(cb), "+s"(wb)); } SBAR(); } } while (0)
    KV_RW(0, 0, 0); KV_RW(0, 0, 1); KV_RW(0, 0, 2); KV_RW(0, 0, 3); KV_RC(0, 0); KV_RC(0, 1); KV_RC(0, 2); KV_RC(0, 3); SBAR();
#pragma unroll
    for (int kt = 0; kt < 16; ++kt) {
        KV_MMA1(kt & 1, 0); KV_RC(kt, 4); KV_RC(kt, 5); SBAR(); if (kt) KV_PIECE(kt + 4, 2);
        KV_MMA1(kt & 1, 1); KV_RC(kt, 6); KV_RC(kt, 7); SBAR();
        KV_MMA1(kt & 1, 2); if (kt) KV_PIECE(kt + 4, 3);
        KV_MMA1(kt & 1, 3);
        if (PART == 0 && kt == 11) {
            w0_check4(fl, ws + WS_LNF, lane * 8, tag, wid);
            w0_flag(ws + WS_CTR, tag - (1ull << 62) + 1, wid | blockIdx.x, vzero);
        }
        if (kt == 0) wait_bar<8>(); else wait_bar<12>();
        if (kt == 0) { KV_PIECE(4, 0); KV_PIECE(4, 1); KV_PIECE(4, 2); KV_PIECE(4, 3); }
        if (kt == 3) tag = w0_tag(tk);
        if (PART == 0 && kt == 3) w0_flag(ws + WS_LNF + blockIdx.x * 8, tag, wid, vzero);
        if (PART == 0 && kt == 6) w0_load4(fl, ws + WS_LNF, lane * 8, wid);
        const int nb = (kt + 1) & 1; const bool nx = kt < 15;
#define KV_OUT(t) do { if (PART == 0 && !nx) { store16_sc1(ob, vlin + (t) * 4096, pack2(acc[t][0], acc[t][1])); store16_sc1(ob, vlin + (t) * 4096 + 1024, pack2(acc[t][2], acc[t][3])); SBAR(); } } while (0)
        if (!nx) { KV_PIECE(kt + 5, 0); KV_PIECE(kt + 5, 1); KV_PIECE(kt + 5, 2); KV_PIECE(kt + 5, 3); }
        KV_MMA1(kt & 1, 4); if (nx) { KV_RC(kt + 1, 0); KV_RW(nb, kt + 1, 0); SBAR(); KV_PIECE(kt + 5, 0); } KV_OUT(0);
        KV_MMA1(kt & 1, 5); if (nx) { KV_RW(nb, kt + 1, 1); KV_RW(nb, kt + 1, 2); SBAR(); } KV_OUT(1);
        KV_MMA1(kt & 1, 6); if (nx) { KV_RW(nb, kt + 1, 3); KV_RC(kt + 1, 1); SBAR(); KV_PIECE(kt + 5, 1); } KV_OUT(2);
        KV_MMA1(kt & 1, 7); if (nx) { KV_RC(kt + 1, 2); KV_RC(kt + 1, 3); SBAR(); } KV_OUT(3);
        if (kt < 8) touch_if(td, wid, kt, toff0, tp0);
        if (PART == 0 && (kt == 8 || kt == 9)) { const unsigned l_ = lane_now(); unsigned o_ = (blockIdx.x & 7) * 384 + (l_ & 3) * 128; if (o_ > FF * 2 - 4) o_ = FF * 2 - 4;
            touch_if(td, wid, kt - 7, ((blockIdx.x >> 3) * 16 + (l_ >> 2)) * (FF * 2) + o_, kt == 8 ? wgp : wup); }
    }
    asm volatile("" :: "v"(td));
#undef KV_RW
#undef KV_RC
#undef KV_MMA1
#undef KV_PIECE
}

template <int GU> DI void q_loop(lptr lds, unsigned lds0, QSrc& qs, const char* kgw, const char* vgw, char* ws, u64 tag, int head, char* ob, u32x4 (&pk)[16],
                                 int wid, int lane, f32x4 (&acc)[8][4]) {
    constexpr int NT = GU ? 6 : 8;
    const int g = lane >> 4, q = (lane & 15) >> 2, p4 = lane & 3;
    const unsigned vlin = lane * 16, vzero = 0;
    lptr xa = lds + (wid & 3) * 4096 + lane * 16;
    lptr wlo[4], whi[4];
#pragma unroll
    for (int t = 0; t < 4; ++t) { lptr w_ = GU ? lds + 24576 + (16 * (g >> 1) + 4 * (g & 1) + q) * 96 + p4 * 8 : lds + 16384 + (8 * g + q) * 256 + ((t ^ q) * 32) + p4 * 8;
        wlo[t] = GU ? w_ : w_ + (g & 1) * 128; whi[t] = w_ + (128 - (g & 1) * 128); }
    bf16x8 xf[2][4], wf[NT];
    Fl4 fl = {};
#define Q_RX(buf, kt, i) do { xf[buf][i] = ldsr(xa + QSLOT(kt) + (i) * 1024); } while (0)
#define Q_RW(kt, t) do { if ((t) < NT) wf[(t) < NT ? (t) : 0] = GU ? ldtr2(wlo[0] + QSLOT(kt) + ((t) / 3) * 3072 + ((t) % 3) * 32, 768) : ldtr2(((t) < 4 ? wlo[(t) & 3] : whi[(t) & 3]) + QSLOT(kt), 1024); } while (0)
#define Q_MMA1(buf, t) do { _Pragma("unroll") for (int i = 0; i < 4; ++i) acc[t][i] = MFMA(wf[(t) < NT ? (t) : 0], xf[buf][i], acc[t][i]); SBAR(); } while (0)
#define Q_NEXT(kt, p) do { if ((kt) < 16) Q_PIECE(kt, p); else if ((kt) >= 18) { dma16s(lds0 + ((kt) - 18) * 32768 + wid * 4096 + (p) * 1024, vlin + (p) * 1024, (kt) == 18 ? kgw : (kt) == 19 ? vgw : kgw + 16384); SBAR(); } } while (0)
    constexpr int H = NT / 2;
    Q_RX(0, 0, 0); Q_RX(0, 0, 1); Q_RX(0, 0, 2); Q_RX(0, 0, 3); Q_RW(0, 0); Q_RW(0, 1); Q_RW(0, 2); if (!GU) Q_RW(0, 3); SBAR();
#pragma unroll
    for (int kt = 0; kt < 16; ++kt) {
        Q_MMA1(kt & 1, 0); Q_RW(kt, H); Q_RW(kt, H + 1); SBAR(); if (kt) Q_NEXT(kt + 4, 2);
        Q_MMA1(kt & 1, 1); Q_RW(kt, H + 2); Q_RW(kt, H + 3); SBAR();
        Q_MMA1(kt & 1, 2); if (kt) Q_NEXT(kt + 4, 3);
        if (!GU) Q_MMA1(kt & 1, 3);
        if (!GU && kt == 13) asm volatile(W0_BEGIN "s_waitcnt vmcnt(0)\n\t" W0_END :: [wid] "s"(wid) : "memory", "scc");
        if (!GU && kt == 13) w0_check1(fl, ws + WS_KVF + head * 128, (lane_now() & 7) * 8, tag, wid);
        { constexpr int D = GU ? 2 : 1, I = GU ? 0 : 8;
          if (kt <= 3) wait_bar_n(12 + I + D * kt); else if (kt <= 8) wait_bar_n(12 + 3 * D); else if (kt <= 11) wait_bar_n(12 + D * (11 - kt)); else if (kt == 12) wait_bar<8>(); else if (kt <= 14) wait_bar<4>(); else asm volatile("s_waitcnt lgkmcnt(0)\n\ts_barrier" ::: "memory"); }
        if (!GU && kt == 11) w0_flag(ws + WS_KVF + head * 128 + ((blockIdx.x >> 3) & 7) * 8, tag, wid, vzero);
        if (!GU && kt == 12) w0_load1(fl, ws + WS_KVF + head * 128, (lane_now() & 7) * 8, wid);
        const int nb = (kt + 1) & 1; const bool nx = kt < 15;
#define Q_OUT(i, e) asm volatile("ds_write_b128 %0, %1 offset:%2" :: "v"(qx), "v"(pack2(acc[2 * (e)][i], acc[2 * (e) + 1][i])), "i"((i) * 4096 + (e) * 1024) : "memory")
        const unsigned qx = lds0 + 3 * 32768 + (wid & 3) * 16384 + (nx ? 0 : lane_now() * 16);
        Q_MMA1(kt & 1, H);     if (!GU && !nx) { Q_OUT(0, 0); Q_OUT(1, 0); SBAR(); }
        if (nx) { Q_RW(kt + 1, 0); Q_RX(nb, kt + 1, 0); Q_RX(nb, kt + 1, 1); SBAR(); } Q_NEXT(kt + 5, 0);
        Q_MMA1(kt & 1, H + 1); if (!GU && !nx) { Q_OUT(2, 0); Q_OUT(3, 0); SBAR(); }
        if (nx) { Q_RX(nb, kt + 1, 2); Q_RX(nb, kt + 1, 3); Q_RW(kt + 1, 1); SBAR(); }
        Q_MMA1(kt & 1, H + 2); if (!GU && !nx) { Q_OUT(0, 1); Q_OUT(1, 1); SBAR(); }
        if (nx) { Q_RW(kt + 1, 2); SBAR(); } Q_NEXT(kt + 5, 1);
        if (!GU) { Q_MMA1(kt & 1, H + 3); if (nx) { Q_RW(kt + 1, 3); SBAR(); } }
        if (!nx) { Q_NEXT(kt + 5, 2); Q_NEXT(kt + 5, 3); }
        if (!GU && !nx) { Q_OUT(2, 1); Q_OUT(3, 1);
#pragma unroll
            for (int i = 0; i < 4; ++i) { Q_OUT(i, 2); Q_OUT(i, 3); } }
        if (kt < 8) { if (GU) { const unsigned o_ = (kt >> 1) * 8192 + (kt & 1) * 2048; store16_sc1(ob, vlin + o_, pk[2 * kt]); store16_sc1(ob, vlin + o_ + 1024, pk[2 * kt + 1]); }
                      else store16_sc1(ob, vlin + (4 + (kt >> 1)) * 4096 + (kt & 1) * 1024, pk[kt]); SBAR(); }
    }
#undef Q_RX
#undef Q_RW
#undef Q_MMA1
#undef Q_NEXT
}

#define F2AT 1
struct BMap { int sp, rb, cb, xr; };
DI BMap bmap() { const int b = blockIdx.x, idx = b >> 3; BMap m; m.sp = (b & 7) >> 1; m.rb = 8 * (b & 1) + (idx >> 2); m.cb = idx & 3; m.xr = m.rb < 8 ? m.rb : 15 - m.rb; return m; }
DI unsigned b_voffw(int lane, int wr, int cb, int h) { const int wrow = 4 * h + (lane >> 4); return (8 * wr + wrow) * 1024 + ((((lane & 15) >> 1) ^ wrow) * 32) + (lane & 1) * 16 + cb * 256; }
DI void out_phase(const u16* __restrict__ x, const u16* __restrict__ wo, const u16* __restrict__ wd, char* __restrict__ ws, u16* __restrict__ out, lptr lds, unsigned lds0, u64 tag, int wid) {
    const unsigned lane = lane_now();
    const int b = blockIdx.x, idx = b >> 3; const BMap bm = bmap(); const int sp = bm.sp, rb = bm.rb, cb = bm.cb, xr = bm.xr;
    const int gg = wid >> 2, wr = wid & 3, g = lane >> 4, q = (lane & 15) >> 2, p4 = lane & 3;
    const unsigned vzero = 0, vlin = lane * 16, voffw0 = b_voffw(lane, wr, cb, 0), voffw1 = b_voffw(lane, wr, cb, 1);

    __builtin_amdgcn_s_setprio(0);
    Fl4 fl = {};
    const char* ab = ws + WS_CTX + (size_t)(8 * rb + 2 * wr) * CTX_KS * 1024;
    const unsigned dst = lds0 + gg * 16384 + wr * 2048;
    lptr aa = lds + gg * 16384 + (wr >> 1) * 4096 + lane * 16;
    lptr ah = lds + gg * 16384 + (wr >> 1) * 4096 + (lane & 31) * 16 + (lane >> 5) * 8;
    lptr wa[4];
#pragma unroll
    for (int t = 0; t < 4; ++t) wa[t] = lds + gg * 16384 + 8192 + (4 * g + q) * 256 + (((4 * (wr & 1) + t) ^ ((4 * g + q) & 7)) * 32) + p4 * 8;
    f32x4 acc[4][4];
    const char *an, *wn;
#define B_SLOT(st) ((((st) % 5) == 0 ? 1 : ((st) % 5) == 1 ? 3 : ((st) % 5) == 2 ? 4 : ((st) % 5) == 3 ? 0 : 2) * 32768)
#define B_ADDR(st) do { const int kk_ = (st) < 6 ? 12 * sp + 2 * (st) + gg : 32 * sp + 2 * (st) + gg - 12; \
        an = ab + (size_t)(((st) < 6 ? 128 : 0) + kk_) * 1024; wn = (st) < 6 ? (const char*)wd + (size_t)(kk_ < 43 ? kk_ : 0) * 32768 : (const char*)wo + (size_t)kk_ * 32768; } while (0)
#define B_PIECE(st, p) do { const unsigned d_ = dst + B_SLOT(st); if ((st) < 22) { if ((p) == 0) dma16s(d_, vlin, an); else if ((p) == 1) dma16s(d_ + 1024, vlin + CTX_KS * 1024, an); \
        else if ((p) == 2) dma16(d_ + 8192, voffw0, wn); else dma16(d_ + 8192 + 1024, voffw1, wn); SBAR(); } } while (0)
#define B_STAGE(st) do { B_ADDR(st); B_PIECE(st, 0); B_PIECE(st, 1); B_PIECE(st, 2); B_PIECE(st, 3); } while (0)
    wait_bar<0>();

    w0_flag(ws + WS_F2 + ((idx & 7) * 32 + (b & 7) * 4 + (idx >> 3)) * 8, tag, wid, vzero);
    B_STAGE(2); B_STAGE(3); B_STAGE(4);
    bf16x8 af[2][4], wf[2][4];
#define B_LD8(p) (*(LAS bf16x4*)(p))
#define B_RD1(st, t) do { if ((st) < 22) { wf[(st) & 1][t] = ldtr2(wa[t] + B_SLOT(st), 4096); \
        if ((st) < 6) af[(st) & 1][t] = __builtin_shufflevector(B_LD8(ah + B_SLOT(st) + (t) * 1024), B_LD8(ah + B_SLOT(st) + (t) * 1024 + 512), 0, 1, 2, 3, 4, 5, 6, 7); \
        else af[(st) & 1][t] = ldsr(aa + B_SLOT(st) + (t) * 1024); SBAR(); } } while (0)
#define B_MMA1(st, t) do { _Pragma("unroll") for (int p = 0; p < 4; ++p) acc[p][t] = MFMA(wf[(st) & 1][t], af[(st) & 1][p], (st) == 0 ? f32x4{} : acc[p][t]); SBAR(); } while (0)

    B_RD1(0, 0); B_RD1(0, 1); B_RD1(0, 2); B_RD1(0, 3);
    const size_t go = (size_t)(128 * rb + 64 * (wr >> 1) + 32 * gg + 16 * (sp >> 1) + (lane & 15)) * DM + 128 * cb + 64 * (wr & 1) + 32 * (sp & 1) + 16 * (g & 1) + 8 * (g >> 1);
    u32x4 xv; u16* op = out + go; asm volatile("" : "+v"(op));
#define B_STEP(st) do { \
        if ((st) == F2AT) { if (wid == 0) asm volatile("s_waitcnt vmcnt(%0)" :: "i"(4 * (F2AT == 1)) : "memory"); w0_check1(fl, ws + WS_F2 + xr * 256 + sp * 64, (lane & 7) * 8, tag, wid); } \
        if ((st) >= 2 && (st) <= F2AT) wait_bar<((st) <= 4 ? 4 * (4 - (st)) : 0)>(); else if ((st) <= 17) wait_bar<12>(); else if ((st) == 18) wait_bar<8>(); else if ((st) == 19) wait_bar<4>(); else if ((st) == 20) wait_bar<0>(); \
        if ((st) == F2AT - 1) w0_load1(fl, ws + WS_F2 + xr * 256 + sp * 64, (lane & 7) * 8, wid); \
        if ((st) == F2AT) { if (F2AT > 1) B_STAGE(6); if (F2AT > 2) B_STAGE(7); if (F2AT > 3) B_STAGE(8); } \
        if ((st) == 16) asm volatile("global_load_dwordx4 %0, %1, off" : "=v"(xv) : "v"(x + go) : "memory"); \
        const bool go_ = (st) == 0 || (st) >= F2AT; B_ADDR((st) + 5); \
        B_MMA1(st, 0); B_RD1((st) + 1, 0); B_RD1((st) + 1, 1); if (go_) B_PIECE((st) + 5, 0); \
        B_MMA1(st, 1); B_RD1((st) + 1, 2); B_RD1((st) + 1, 3); if (go_) B_PIECE((st) + 5, 1); \
        B_MMA1(st, 2); if (go_) B_PIECE((st) + 5, 2); \
        B_MMA1(st, 3); if (go_) B_PIECE((st) + 5, 3); } while (0)
    B_STEP(0); B_STEP(1); B_STEP(2); B_STEP(3); B_STEP(4); B_STEP(5); B_STEP(6); B_STEP(7); B_STEP(8); B_STEP(9); B_STEP(10);
    B_STEP(11); B_STEP(12); B_STEP(13); B_STEP(14); B_STEP(15); B_STEP(16); B_STEP(17); B_STEP(18); B_STEP(19); B_STEP(20);
    f32x4 sum[2][4];
    {
        const unsigned sw0 = (wid & 4) * 16384 + (wid & 3) * 8192 + vlin, mine = lds0 + sw0; lptr theirs = lds + (sw0 ^ 65536);
#define B_LAST(G) do { \
        _Pragma("unroll") for (int i = 0; i < 2; ++i) _Pragma("unroll") for (int t = 0; t < 4; ++t) { acc[2 * (1 - (G)) + i][t] = MFMA(wf[1][t], af[1][2 * (1 - (G)) + i], acc[2 * (1 - (G)) + i][t]); SBAR(); } \
        _Pragma("unroll") for (int i = 0; i < 2; ++i) _Pragma("unroll") for (int t = 0; t < 4; ++t) { sum[i][t] = MFMA(wf[1][t], af[1][2 * (G) + i], acc[2 * (G) + i][t]); SBAR(); \
            if (4 * i + t >= 2) asm volatile("ds_write_b128 %0, %1 offset:%2" :: "v"(mine), "v"(acc[2 * (1 - (G)) + ((4 * i + t - 2) >> 2)][(4 * i + t - 2) & 3]), "i"((4 * i + t - 2) * 1024) : "memory"); } \
        asm volatile("ds_write_b128 %0, %1 offset:6144\n\tds_write_b128 %0, %2 offset:7168" :: "v"(mine), "v"(acc[2 * (1 - (G)) + 1][2]), "v"(acc[2 * (1 - (G)) + 1][3]) : "memory"); } while (0)
        if (gg) B_LAST(1); else B_LAST(0);

        asm volatile("s_waitcnt lgkmcnt(0)\n\ts_barrier" : "+v"(xv) :: "memory");
#pragma unroll
        for (int i = 0; i < 2; ++i)
#pragma unroll
            for (int t = 0; t < 4; ++t) sum[i][t] += *(LAS f32x4*)(theirs + (4 * i + t) * 1024);
    }

    const int tile = rb * 4 + cb;
    char* slab = ws + WS_PART + rfl(tile * 4 * 32768 + wid * 4096);
    f32x4 own[2] = {sum[0][0], sum[0][1]};
#pragma unroll
    for (int e = 0; e < 4; ++e) {
#pragma unroll
        for (int i = 0; i < 2; ++i)
#pragma unroll
            for (int c_ = 0; c_ < 4; ++c_) own[i][c_] = __builtin_unpredictable(e == sp) ? sum[e >> 1][2 * (e & 1) + i][c_] : own[i][c_];
        asm volatile("s_cmp_eq_u32 %3, %4\n\ts_cbranch_scc1 SK%=\n\tglobal_store_dwordx4 %0, %1, %2 sc1\n\ts_nop 1\n\tSK%=:"
                     :: "v"(e * 1024 + vlin), "v"(pack2(sum[e >> 1][2 * (e & 1)], sum[e >> 1][2 * (e & 1) + 1])), "s"(slab + sp * 32768), "s"(sp), "i"(e) : "memory", "scc"); }

    asm volatile("s_waitcnt vmcnt(0)\n\ts_barrier" ::: "memory");

    const char* xf = ws + WS_FLAG + rfl(tile * 512);
    w0_flag(xf + sp * 128, tag, wid, vzero);
    w0_load1(fl, xf, (lane & 3) * 128, wid);
    if (wid == 0) asm volatile("s_waitcnt vmcnt(0)" ::: "memory");
    w0_check1(fl, xf, (lane & 3) * 128, tag, wid);
    asm volatile("s_barrier" ::: "memory");

    u32x4 pt[3];
#pragma unroll
    for (int o = 0; o < 3; ++o) asm volatile("global_load_dwordx4 %0, %1, %2 sc1" : "=v"(pt[o]) : "v"(sp * 1024 + vlin), "s"(slab + (o + (o >= sp)) * 32768) : "memory");
    asm volatile("s_waitcnt vmcnt(0)" : "+v"(pt[0]), "+v"(pt[1]), "+v"(pt[2]) :: "memory");

    float r8[8];
#pragma unroll
    for (int o = 0; o < 3; ++o)
#pragma unroll
        for (int i = 0; i < 2; ++i) own[i] += (f32x4){bflo(pt[o][2 * i]), bfhi(pt[o][2 * i]), bflo(pt[o][2 * i + 1]), bfhi(pt[o][2 * i + 1])};
#pragma unroll
    for (int j = 0; j < 4; ++j) {
        const auto sw = __builtin_amdgcn_permlane16_swap(__float_as_uint(own[0][j]), __float_as_uint(own[1][j]), false, false);
        r8[j] = __uint_as_float(sw[0]); r8[4 + j] = __uint_as_float(sw[1]);
    }
    const u32x4 res = {cvtpk(r8[0] + bflo(xv[0]), r8[1] + bfhi(xv[0])), cvtpk(r8[2] + bflo(xv[1]), r8[3] + bfhi(xv[1])), cvtpk(r8[4] + bflo(xv[2]), r8[5] + bfhi(xv[2])), cvtpk(r8[6] + bflo(xv[3]), r8[7] + bfhi(xv[3]))};
    asm volatile("k1_code_end:\n\tglobal_store_dwordx4 %0, %1, off" :: "v"(op), "v"(res) : "memory");

}

__global__ __launch_bounds__(512, 2) void k1_main(const u16* __restrict__ x, const u16* __restrict__ c, const u16* __restrict__ wk, const u16* __restrict__ wv,
                                                  const u16* __restrict__ wq, const u16* __restrict__ wg, const u16* __restrict__ wu, const u16* __restrict__ wo, const u16* __restrict__ wd, char* __restrict__ ws, u16* __restrict__ out) {
    extern __shared__ __attribute__((aligned(16))) char smem[];
    lptr lds = (lptr)smem; const unsigned lds0 = (unsigned)(size_t)lds;
    const int tid = threadIdx.x, lane = tid & 63, wid = rfl(tid >> 6);
    const int b = blockIdx.x, idx = b >> 3, head = (b & 7) * 4 + (idx >> 3), xl = idx & 7;
    const int wr = wid & 3;

    const char* pc; unsigned clen;
    asm volatile("s_getpc_b64 %0\nk1_code_ref:\n\ts_mov_b32 %1, k1_code_end-k1_code_ref" : "=s"(pc), "=s"(clen));
    unsigned td = 0;
    {
        const unsigned o_ = idx * 8192 + lane * 128; touch_lt(td, idx + (7 - wid) * 64, CODEKB / 8, o_ < clen ? o_ : clen, pc);
    }
    const int xrow = 8 * b + wid; u32x4 xv;
    asm volatile("global_load_dwordx4 %0, %1, %2" : "=v"(xv) : "v"(lane * 16), "s"((const char*)x + (size_t)xrow * 1024) : "memory");

    asm volatile("s_barrier" ::: "memory");
    const unsigned voffc = (32 * wid + (lane >> 2)) * 1024 + (((lane & 3) ^ ((lane >> 5) * 3)) * 16);
    const int part = wid >> 2, qq = lane >> 4;
    const unsigned voffw = (8 * (wid & 3) + qq) * 8192 + (((((lane & 15) >> 1) ^ (qq | ((wid & 1) << 2))) * 16 + (lane & 1) * 8) * 2);
    const char* cb = (const char*)c + (size_t)xl * 256 * 1024;
    const char* wb = (const char*)(part ? wv : wk) + head * 256;
#define KV_STAGE0(kt) do { const unsigned dstc = lds0 + wid * 2048 + KSLOT(kt), dstw = dstc + 16384; \
        dma16(dstc, voffc, cb); dma16(dstc + 1024, voffc + 16384, cb); dma16(dstw, voffw, wb); dma16(dstw + 1024, voffw + 32768, wb); \
        cb += 64; wb += 32 * 8192; asm volatile("" : "+s"(cb), "+s"(wb)); } while (0)
    KV_STAGE0(0);

    asm volatile("s_barrier" ::: "memory");
    const unsigned wl = (lane >> 3) * 8192 + (b & 7) * 1024 + (lane & 7) * 128;
    touch_lt(td, (wid / 3) * idx + (wid / 5) * 64, 24, wid < 3 ? (idx * 64 + lane) * 1024 + (wid + 1) * 128 : (32 + (wid - 3) * 96 + (idx % 12) * 8) * 8192 + wl, uptr(wid < 3 ? c : idx < 12 ? wk : wv));
    KV_STAGE0(1);
    asm volatile("s_waitcnt vmcnt(%1)" : "+v"(xv) : "i"(8) : "memory");

    {
        float v[8];
#pragma unroll
        for (int j = 0; j < 4; ++j) { v[2 * j] = bflo(xv[j]); v[2 * j + 1] = bfhi(xv[j]); }
        float s = 0;
#pragma unroll
        for (int j = 0; j < 8; ++j) s += v[j];
        const float m = wave_sum(s) * (1.0f / DM); float qv = 0;
#pragma unroll
        for (int j = 0; j < 8; ++j) { v[j] -= m; qv += v[j] * v[j]; }
        const float rs = __builtin_amdgcn_rsqf(wave_sum(qv) * (1.0f / DM) + 1e-5f) * QSCALE;
        u32x4 o4 = {cvtpk(v[0] * rs, v[1] * rs), cvtpk(v[2] * rs, v[3] * rs), cvtpk(v[4] * rs, v[5] * rs), cvtpk(v[6] * rs, v[7] * rs)};
        *(LAS u32x4*)(lds + KSLOT(4) + lane * 128 + wid * 16) = o4;
    }
    QSrc qs;
    qs.xb = ws + WS_XN + (size_t)((wid < 4 ? 8 * xl : 8 * (15 - xl) - 8) + 2 * wid) * 16384;
    qs.voffq = (4 * wid + qq) * 8192 + (((((lane & 15) >> 1) ^ (qq | (((wid >> 1) & 1) << 2))) * 16 + (lane & 1) * 8) * 2);
    qs.wqb = (const char*)wq + head * 256;
    { const int pg = wid < 6 ? wid : wid - 2, f0 = head < 29 ? 48 * head : 0;
      const int o = 1024 * (pg >= 3 ? pg - 3 : pg) + 16 * lane, rs = o / 96, c6 = (o % 96) >> 4, row = (rs & ~12) | ((rs & 4) << 1) | ((rs & 8) >> 1);
      int col = f0 + 8 * c6; if (col > FF - 8) col = FF - 8; qs.voffg = row * (FF * 2) + col * 2;
      qs.wgb = (const char*)(pg >= 3 ? wu : wg); qs.dstg = lds0 + 24576 + pg * 1024; }
    const unsigned toff0 = (wid & 1) ? (idx * 64 + lane) * 1024 + (4 + (wid >> 1)) * 128 : wid < 4 ? (224 + 64 * wid + (idx & 15) * 8) * 8192 + wl : ((wid - 4) * 128 + idx * 8) * 8192 + wl;
    const void* tp0 = uptr((wid & 1) ? c : wid < 4 ? (idx < 16 ? wk : wv) : wq);

    wait_bar<4>();
    KV_STAGE0(2); KV_STAGE0(3);
    u32x2 tk = {0u, 0u}; u64 tag = 0;
        w0_epoch(tk, ws + WS_CTR, 0, 0);
        {
        const u32x4 o4 = *(LAS u32x4*)(lds + KSLOT(4) + wid * 1024 + lane * 16); const int ch = 8 * wid + (lane >> 3);
        store16_sc1(ws + WS_XN + rfl((blockIdx.x >> 1) * 16384 + (blockIdx.x & 1) * 128), (ch >> 2) * 1024 + (ch & 3) * 256 + (lane & 7) * 16, o4);
        }

    f32x4 acc[8][4];
#pragma unroll
    for (int i = 0; i < 8; ++i)
#pragma unroll
        for (int j = 0; j < 4; ++j) acc[i][j] = f32x4{};
    const int kh = (wid >> 1) & 1, dh = wid & 1;
    u32x4 pk[16];
    const char* kgw = ws + WS_K + (size_t)head * 512 * 1024 + wr * 4096; const char* vgw = ws + WS_V + (size_t)head * 512 * 1024 + wr * 4096;
    const int rb0 = (wr < 2 ? 8 * xl : 8 * (15 - xl) - 8) + 4 * wr;
    if (part == 0) {
        char* ob = ws + WS_K + ((size_t)(head * 128 + 16 * xl + 8 * kh) * 4 + 2 * dh) * 1024;
        kv_loop<0>(lds, lds0, cb, wb, voffc, voffw, qs, ws, tk, tag, toff0, tp0, td, wg, wu, ob, wid, lane, acc);

#pragma unroll
        for (int t = 4; t < 8; ++t)
#pragma unroll
            for (int e = 0; e < 2; ++e) pk[2 * (t - 4) + e] = pack2(acc[t][2 * e], acc[t][2 * e + 1]);
#pragma unroll
        for (int i = 0; i < 8; ++i)
#pragma unroll
            for (int j = 0; j < 4; ++j) acc[i][j] = f32x4{};

        q_loop<0>(lds, lds0, qs, kgw, vgw, ws, tag, head, ob, pk, wid, lane, acc);

    } else {
        char* ob = ws + WS_V + ((size_t)(head * 64 + 8 * xl + 4 * kh) * 8 + 4 * dh) * 1024;
        kv_loop<1>(lds, lds0, cb, wb, voffc, voffw, qs, ws, tk, tag, toff0, tp0, td, wg, wu, ob, wid, lane, acc);

#pragma unroll
        for (int u = 0; u < 4; ++u)
#pragma unroll
            for (int dt = 0; dt < 4; ++dt) pk[4 * u + dt] = pack2(acc[2 * u][dt], acc[2 * u + 1][dt]);
#pragma unroll
        for (int i = 0; i < 8; ++i)
#pragma unroll
            for (int j = 0; j < 4; ++j) acc[i][j] = f32x4{};

        q_loop<1>(lds, lds0, qs, kgw, vgw, ws, tag, head, ob, pk, wid, lane, acc);

        const unsigned l2 = lane_now();
#pragma unroll
        for (int t = 0; t < 3; ++t) {
            const int T = 3 * head + t; char* hb = ws + WS_CTX + rfl((rb0 * CTX_KS + 128 + (T >> 1)) * 1024 + (T & 1) * 512);
#pragma unroll
            for (int i = 0; i < 4; i += 2) { unsigned h2[2][2];
#pragma unroll
                for (int k = 0; k < 2; ++k) { const f32x4 gt = acc[t][i + k], up = acc[3 + t][i + k] * (1.0f / (QSCALE * QSCALE));
                    h2[k][0] = cvtpk(__builtin_fmaxf(gt[0], 0.f) * up[0], __builtin_fmaxf(gt[1], 0.f) * up[1]); h2[k][1] = cvtpk(__builtin_fmaxf(gt[2], 0.f) * up[2], __builtin_fmaxf(gt[3], 0.f) * up[3]); }
                const auto s0 = __builtin_amdgcn_permlane32_swap(h2[0][0], h2[1][0], false, false), s1 = __builtin_amdgcn_permlane32_swap(h2[0][1], h2[1][1], false, false);
                const unsigned keep = T < 86 ? ~0u : 0u;
                store16_sc1(hb, (l2 & 31) * 16 + (i + (l2 >> 5)) * (CTX_KS * 1024), (u32x4){s0[0] & keep, s1[0] & keep, s0[1] & keep, s1[1] & keep}); }
        }
    }

    {
        const int n = idx * 8 + wid, ln = lane_now(); const BMap bm = bmap(); (void)n;
        unsigned toff1 = 0; const void* tp1 = x;
        { const int m = b * 8 + wid; if (m < 512) { toff1 = m * 8192 + ln * 128; tp1 = wo; } else if (m < 684) { toff1 = (m - 512) * 8192 + ln * 128; tp1 = wd; } }
        const char* ctxL = ws + WS_CTX + ((size_t)(8 * xl + 2 * wr) * CTX_KS + 4 * head) * 1024;
        const char* ctxH = ws + WS_CTX + ((size_t)(8 * (15 - xl) + 2 * wr) * CTX_KS + 4 * head) * 1024;
        const char* f1 = ws + WS_F1 + (xl * 32 + head) * 8;
        const char* f1r = ws + WS_F1 + bm.xr * 256;
        const char* an0 = ws + WS_CTX + ((size_t)(8 * bm.rb + 2 * wr) * CTX_KS + 128 + 12 * bm.sp) * 1024; const char* wn0 = (const char*)wd + (size_t)12 * bm.sp * 32768;
#define ATTN_OPS "s"(kgw), "s"(vgw), "s"(ctxL), "s"(ctxH), "s"(xl), "s"(wid), "s"(lds0), "s"(tp1), "v"(toff1), "s"(f1), "s"((unsigned)tag), "s"((unsigned)(tag >> 32)), \
        "s"(f1r), "s"(an0), "s"(wn0), "v"(b_voffw(ln, wr, bm.cb, 0)), "v"(b_voffw(ln, wr, bm.cb, 1))
        asm volatile(ATTN_ASM :: ATTN_OPS : ATTN_CLOBBERS);
    }
    out_phase(x, wo, wd, ws, out, lds, lds0, tag, wid);
}

extern "C" void kernel_launch(void* const* d_in, const int* in_sizes, int n_in, void* d_out, int out_size, void* d_ws, size_t ws_size, hipStream_t stream) {
    const u16* x = (const u16*)d_in[0]; const u16* c = (const u16*)d_in[1]; const u16* wq = (const u16*)d_in[2]; const u16* wk = (const u16*)d_in[3];
    const u16* wv = (const u16*)d_in[4]; const u16* wo = (const u16*)d_in[5]; const u16* wg = (const u16*)d_in[7]; const u16* wu = (const u16*)d_in[8]; const u16* wd = (const u16*)d_in[9];
    (void)in_sizes; (void)n_in; (void)out_size; (void)ws_size; static_assert(WS_END < ((size_t)256 << 20), "workspace");
    (void)hipFuncSetAttribute((const void*)k1_main, hipFuncAttributeMaxDynamicSharedMemorySize, 163840);
    k1_main<<<256, 512, 163840, stream>>>(x, c, wk, wv, wq, wg, wu, wo, wd, (char*)d_ws, (u16*)d_out);
}
```
